```python
import torch

def _softmax(x, axis=-1):
    return torch.softmax(x, dim=axis)

def _gelu(x):
    return torch.nn.functional.gelu(x, approximate='tanh')

def _silu(x):
    return torch.nn.functional.silu(x)

def _sigmoid(x):
    return torch.sigmoid(x)

def _layernorm(x, axis=-1, eps=1e-05):
    m = x.mean(dim=axis, keepdim=True)
    v = x.var(dim=axis, keepdim=True, unbiased=False)
    return (x - m) / torch.sqrt(v + eps)

def _scatter_add(tgt, idx, src, axis=0):
    return tgt.clone().index_add_(axis, idx, src)

def _segment_sum(src, seg, n):
    out = torch.zeros((n,) + src.shape[1:], dtype=src.dtype)
    return out.index_add_(0, seg, src)

def _pad(x, pw):
    flat = tuple((v for pair in reversed(pw) for v in pair))
    return torch.nn.functional.pad(x, flat)
kernel_name = 'standalone__video_timesformer__vision'

def setup_inputs():

    _w = lambda a: (lambda t: t.to(torch.bfloat16) if t.is_floating_point() else t)(torch.as_tensor(a))
    T = 7
    p = 14
    HW = 384 // p * p
    N = (HW // p) ** 2
    C = 384
    return {'x': _w((lambda *s: torch.randn(*s, dtype=torch.bfloat16) * 0.1)(1, T, N, C)), 'wqkv_t': _w((lambda *s: torch.randn(*s, dtype=torch.bfloat16) * 0.1)(C, 3 * C)), 'wo_t': _w((lambda *s: torch.randn(*s, dtype=torch.bfloat16) * 0.1)(C, C)), 'wqkv_s': _w((lambda *s: torch.randn(*s, dtype=torch.bfloat16) * 0.1)(C, 3 * C)), 'wo_s': _w((lambda *s: torch.randn(*s, dtype=torch.bfloat16) * 0.1)(C, C))}

def reference(x, wqkv_t, wo_t, wqkv_s, wo_s):
    B, T, N, C = x.shape
    scale = C ** (-0.5)
    xt = (lambda x, *d: x.permute(*d))(x, 0, 2, 1, 3).reshape(B * N, T, C)
    h = _layernorm(xt, axis=-1)
    qkv = h @ wqkv_t
    q, k, v = (qkv[..., :C].clone(), qkv[..., C:2 * C].clone(), qkv[..., 2 * C:].clone())
    a = _softmax(torch.einsum('btc,bsc->bts', q, k) * scale, axis=-1)
    xt = xt + torch.einsum('bts,bsc->btc', a, v) @ wo_t
    x1 = (lambda x, *d: x.permute(*d))(xt.reshape(B, N, T, C), 0, 2, 1, 3)
    xs = x1.reshape(B * T, N, C)
    h = _layernorm(xs, axis=-1)
    qkv = h @ wqkv_s
    q, k, v = (qkv[..., :C].clone(), qkv[..., C:2 * C].clone(), qkv[..., 2 * C:].clone())
    a = _softmax(torch.einsum('bnc,bmc->bnm', q, k) * scale, axis=-1)
    xs = xs + torch.einsum('bnm,bmc->bnc', a, v) @ wo_s
    return xs.reshape(B, T, N, C)
```

```cpp
#include <hip/hip_runtime.h>
#include <hip/hip_bf16.h>

using bf16x8 = __attribute__((ext_vector_type(8))) __bf16;
using bf16x2 = __attribute__((ext_vector_type(2))) __bf16;
using s16x4  = __attribute__((ext_vector_type(4))) short;
using s16x8  = __attribute__((ext_vector_type(8))) short;
using f32x16 = __attribute__((ext_vector_type(16))) float;
using f32x4  = __attribute__((ext_vector_type(4))) float;
using f32x2  = __attribute__((ext_vector_type(2))) float;
using u32x4  = __attribute__((ext_vector_type(4))) unsigned;
typedef __attribute__((address_space(3))) s16x4 lds_s16x4;

#define DI __device__ __forceinline__
#define MFMA(a, b, c) __builtin_amdgcn_mfma_f32_32x32x16_bf16((a), (b), (c), 0, 0, 0)

constexpr int TF = 7, NP = 729, CH = 384;
constexpr int NTILE = 23;
constexpr int WBYTES = 12 * 24 * 1024;
constexpr int TILEB = 24 * 1024;
constexpr size_t OFF_WA_T = 0, OFF_WB_T = WBYTES, OFF_WA_S = 2 * WBYTES, OFF_WB_S = 3 * WBYTES;
constexpr size_t OFF_H2 = 4 * (size_t)WBYTES;
constexpr size_t OFF_X1 = OFF_H2 + (size_t)TF * NTILE * TILEB;

DI int perm23(int r) { return (r & 0x13) | ((r & 4) << 1) | ((r & 8) >> 1); }
DI int slot_off(int ksodd, int h, int r) { return (h * 32 + (r ^ (4 * (2 * ksodd + h)))) * 16; }

DI unsigned pack2(float a, float b) {
  f32x2 p = {a, b};
  return __builtin_bit_cast(unsigned, __builtin_convertvector(p, bf16x2));
}
DI bf16x8 pack8(const f32x16& x, int s) {
  u32x4 p;
  p[0] = pack2(x[8 * s + 0], x[8 * s + 1]); p[1] = pack2(x[8 * s + 2], x[8 * s + 3]);
  p[2] = pack2(x[8 * s + 4], x[8 * s + 5]); p[3] = pack2(x[8 * s + 6], x[8 * s + 7]);
  return __builtin_bit_cast(bf16x8, p);
}
DI float bflo(unsigned u) { return __builtin_bit_cast(float, u << 16); }
DI float bfhi(unsigned u) { return __builtin_bit_cast(float, u & 0xffff0000u); }
DI float rbf(float x) { return bflo(pack2(x, 0.f) & 0xffffu); }
DI float xhalf(float v) {
  return __shfl_xor(v, 32, 64);
}
DI void lds_barrier() { asm volatile("s_waitcnt lgkmcnt(0)\n\ts_barrier" ::: "memory"); }

DI bf16x8 tr_frag(const char* p0, const char* p1) {
  s16x4 lo = __builtin_amdgcn_ds_read_tr16_b64_v4i16((lds_s16x4*)p0);
  s16x4 hi = __builtin_amdgcn_ds_read_tr16_b64_v4i16((lds_s16x4*)p1);
  s16x8 v = __builtin_shufflevector(lo, hi, 0, 1, 2, 3, 4, 5, 6, 7);
  return __builtin_bit_cast(bf16x8, v);
}

__global__ __launch_bounds__(256) void k0_fold(const unsigned short* __restrict__ wqkv_t, const unsigned short* __restrict__ wo_t,
                                               const unsigned short* __restrict__ wqkv_s, const unsigned short* __restrict__ wo_s,
                                               char* __restrict__ ws) {
  __shared__ __attribute__((aligned(16))) char slab[384 * 64];
  const int b = blockIdx.x, prod = b / 36, rem = b % 36, ntile = rem / 3, cg = rem % 3;
  const int tid = threadIdx.x, w = tid >> 6, l = tid & 63, r = l & 31, h = l >> 5;
  const int ctile = 4 * cg + w;
  const unsigned short* wqkv = (prod < 2) ? wqkv_t : wqkv_s;
  const unsigned short* wo = (prod < 2) ? wo_t : wo_s;
  const bool isB = prod & 1;
  const unsigned short* arow = wqkv + (size_t)(32 * ctile + perm23(r)) * 1152 + (isB ? 768 : 0) + 8 * h;
  f32x16 acc = {};
  if (!isB) {
    const unsigned short* brow = wqkv + (size_t)(32 * ntile + perm23(r)) * 1152 + 384 + 8 * h;
    bf16x8 af[24], bfr[24];
#pragma unroll
    for (int ks = 0; ks < 24; ++ks) { af[ks] = *(const bf16x8*)(arow + 16 * ks); bfr[ks] = *(const bf16x8*)(brow + 16 * ks); }
#pragma unroll
    for (int ks = 0; ks < 24; ++ks) acc = MFMA(af[ks], bfr[ks], acc);
    const float sc = 0.05103103630798288f * 1.4426950408889634f;
#pragma unroll
    for (int i = 0; i < 16; ++i) acc[i] *= sc;
  } else {
    bf16x8 af[24];
#pragma unroll
    for (int ks = 0; ks < 24; ++ks) af[ks] = *(const bf16x8*)(arow + 16 * ks);
#pragma unroll
    for (int i = 0; i < 6; ++i) {
      const int piece = tid + 256 * i, e = piece >> 2, q4 = piece & 3;
      *(u32x4*)(slab + e * 64 + q4 * 16) = *(const u32x4*)(wo + (size_t)e * 384 + 32 * ntile + 8 * q4);
    }
    __syncthreads();
    const int blk = (l >> 4) & 1, q = (l & 15) >> 2, p = l & 3;
    const char* tb = slab + (8 * h + q) * 64 + (16 * blk + 8 * (p & 1) + 4 * (p >> 1)) * 2;
#pragma unroll
    for (int ks = 0; ks < 24; ++ks) acc = MFMA(af[ks], tr_frag(tb + ks * 1024, tb + ks * 1024 + 256), acc);
  }
  char* dst = ws + (size_t)prod * WBYTES;
  const int wv = ntile / 3, ti = ntile % 3;
#pragma unroll
  for (int s = 0; s < 2; ++s) {
    const int ks = 2 * ctile + s;
    *(bf16x8*)(dst + (size_t)((wv * 24 + ks) * 3 + ti) * 1024 + l * 16) = pack8(acc, s);
  }
}

DI void partial_stats(const float (&v)[48], float& mean_w, float& m2_w) {
  float s = 0.f;
#pragma unroll
  for (int i = 0; i < 48; ++i) s += v[i];
  s += xhalf(s);
  mean_w = s * (1.f / 96.f);
  float q = 0.f;
#pragma unroll
  for (int i = 0; i < 48; ++i) { const float d = v[i] - mean_w; q += d * d; }
  q += xhalf(q);
  m2_w = q;
}
DI void combine_stats(const f32x2* st, int r, float& mean, float& rstd) {
  const f32x2 a = st[r], b = st[32 + r], c = st[64 + r], d = st[96 + r];
  const float m = 0.25f * (a[0] + b[0] + c[0] + d[0]);
  const float da = a[0] - m, db = b[0] - m, dc = c[0] - m, dd = d[0] - m;
  const float m2 = a[1] + b[1] + c[1] + d[1] + 96.f * (da * da + db * db + dc * dc + dd * dd);
  const float var = m2 * (1.f / 384.f);
  mean = rbf(m);
  rstd = 1.f / rbf(sqrtf(rbf(rbf(var) + 1e-5f)));
}

__global__ __launch_bounds__(256, 1) void k1_temporal(const unsigned short* __restrict__ x, const char* __restrict__ WA,
                                                      const char* __restrict__ WB, char* __restrict__ H2, char* __restrict__ X1) {
  __shared__ __attribute__((aligned(16))) char lds[TILEB + 16384 + 2048];
  char* himg = lds;
  char* red = lds + TILEB;
  f32x2* st = (f32x2*)(lds + TILEB + 16384);
  const int tid = threadIdx.x, w = __builtin_amdgcn_readfirstlane(tid >> 6), l = tid & 63, r = l & 31, h = l >> 5;
  const int pos = r / 7, t = r - 7 * pos, n = 4 * blockIdx.x + pos;
  const bool valid = (r < 28) && (n < NP);
  u32x4 xr[6];
  {
    const unsigned short* xrow = x + ((size_t)t * NP + (valid ? n : 0)) * CH + 96 * w + 8 * h;
#pragma unroll
    for (int i = 0; i < 6; ++i) { xr[i] = *(const u32x4*)(xrow + 16 * i); if (!valid) xr[i] = u32x4{0, 0, 0, 0}; }
  }
  const char* wa = WA + (size_t)w * 72 * 1024 + l * 16;
  const char* wb = WB + (size_t)w * 72 * 1024 + l * 16;
  const int so0 = slot_off(0, h, r), so1 = slot_off(1, h, r);
  bf16x8 hown[6];
  {
    float v[48];
#pragma unroll
    for (int i = 0; i < 6; ++i)
#pragma unroll
      for (int j = 0; j < 4; ++j) { v[8 * i + 2 * j] = bflo(xr[i][j]); v[8 * i + 2 * j + 1] = bfhi(xr[i][j]); }
    float mw, m2;
    partial_stats(v, mw, m2);
    if (h == 0) st[w * 32 + r] = f32x2{mw, m2};
    lds_barrier();
    float mean, rstd;
    combine_stats(st, r, mean, rstd);
#pragma unroll
    for (int i = 0; i < 6; ++i) {
      u32x4 p;
#pragma unroll
      for (int j = 0; j < 4; ++j) p[j] = pack2(rbf(v[8 * i + 2 * j] - mean) * rstd, rbf(v[8 * i + 2 * j + 1] - mean) * rstd);
      hown[i] = __builtin_bit_cast(bf16x8, p);
      *(u32x4*)(himg + (6 * w + i) * 1024 + ((i & 1) ? so1 : so0)) = p;
    }
    lds_barrier();
  }
  f32x16 acc[3] = {};
#pragma unroll
  for (int ks = 0; ks < 24; ++ks) {
    const bf16x8 hf = *(const bf16x8*)(himg + ks * 1024 + ((ks & 1) ? so1 : so0));
#pragma unroll
    for (int i = 0; i < 3; ++i) acc[i] = MFMA(*(const bf16x8*)(wa + (ks * 3 + i) * 1024), hf, acc[i]);
  }
  f32x16 sacc = {};
#pragma unroll
  for (int i = 0; i < 3; ++i)
#pragma unroll
    for (int s = 0; s < 2; ++s) sacc = MFMA(hown[2 * i + s], pack8(acc[i], s), sacc);
#pragma unroll
  for (int g = 0; g < 4; ++g)
    *(f32x4*)(red + w * 4096 + g * 1024 + l * 16) = f32x4{sacc[4 * g], sacc[4 * g + 1], sacc[4 * g + 2], sacc[4 * g + 3]};
  lds_barrier();
  f32x16 sc;
#pragma unroll
  for (int g = 0; g < 4; ++g) {
    f32x4 a = *(const f32x4*)(red + g * 1024 + l * 16);
#pragma unroll
    for (int ww = 1; ww < 4; ++ww) a += *(const f32x4*)(red + ww * 4096 + g * 1024 + l * 16);
    sc[4 * g] = a[0]; sc[4 * g + 1] = a[1]; sc[4 * g + 2] = a[2]; sc[4 * g + 3] = a[3];
  }
  {
    const int lo = 7 * pos;
    float mx = -3.0e38f;
#pragma unroll
    for (int i = 0; i < 16; ++i) {
      const int row = (i & 3) + 8 * (i >> 2) + 4 * h;
      const bool ok = (row >= lo) && (row < lo + 7) && (r < 28);
      sc[i] = ok ? sc[i] : -3.0e38f;
      mx = fmaxf(mx, sc[i]);
    }
    mx = fmaxf(mx, xhalf(mx));
    float sum = 0.f;
#pragma unroll
    for (int i = 0; i < 16; ++i) {
      const float e = (sc[i] > -1.0e38f) ? __builtin_amdgcn_exp2f(sc[i] - mx) : 0.f;
      sc[i] = e; sum += e;
    }
    sum += xhalf(sum);
    const float inv = (sum > 0.f) ? 1.f / sum : 0.f;
#pragma unroll
    for (int i = 0; i < 16; ++i) sc[i] *= inv;
  }
  const bf16x8 pf0 = pack8(sc, 0), pf1 = pack8(sc, 1);
  bf16x8 hm[24];
  {
    const int blk = (l >> 4) & 1, q = (l & 15) >> 2, p = l & 3, hh = p & 1;
    const int xr4 = 4 * (2 * blk + hh);
    const char* tb0 = himg + blk * 1024 + (hh * 32 + ((4 * h + q) ^ xr4)) * 16 + 8 * (p >> 1);
    const char* tb1 = himg + blk * 1024 + (hh * 32 + ((8 + 4 * h + q) ^ xr4)) * 16 + 8 * (p >> 1);
#pragma unroll
    for (int c = 0; c < 12; ++c) {
      f32x16 a = {};
      a = MFMA(tr_frag(tb0 + c * 2048, tb1 + c * 2048), pf0, a);
      a = MFMA(tr_frag(tb0 + c * 2048 + 256, tb1 + c * 2048 + 256), pf1, a);
      hm[2 * c] = pack8(a, 0); hm[2 * c + 1] = pack8(a, 1);
    }
  }
#pragma unroll
  for (int i = 0; i < 3; ++i) acc[i] = f32x16{};
#pragma unroll
  for (int ks = 0; ks < 24; ++ks)
#pragma unroll
    for (int i = 0; i < 3; ++i) acc[i] = MFMA(*(const bf16x8*)(wb + (ks * 3 + i) * 1024), hm[ks], acc[i]);
  {
    float v[48];
    u32x4 x1p[6];
#pragma unroll
    for (int i = 0; i < 6; ++i)
#pragma unroll
      for (int j = 0; j < 4; ++j) {
        const float a = bflo(xr[i][j]) + rbf(acc[i >> 1][8 * (i & 1) + 2 * j]);
        const float b = bfhi(xr[i][j]) + rbf(acc[i >> 1][8 * (i & 1) + 2 * j + 1]);
        const unsigned u = pack2(a, b);
        x1p[i][j] = u;
        v[8 * i + 2 * j] = bflo(u); v[8 * i + 2 * j + 1] = bfhi(u);
      }
    float mw, m2;
    partial_stats(v, mw, m2);
    if (h == 0) st[128 + w * 32 + r] = f32x2{mw, m2};
    lds_barrier();
    float mean, rstd;
    combine_stats(st + 128, r, mean, rstd);
    if (r < 28) {
      const int rr = n & 31;
      const size_t tile = ((size_t)t * NTILE + (n >> 5)) * TILEB + (size_t)(6 * w) * 1024;
      const int d0 = slot_off(0, h, rr), d1 = slot_off(1, h, rr);
#pragma unroll
      for (int i = 0; i < 6; ++i) {
        u32x4 p;
#pragma unroll
        for (int j = 0; j < 4; ++j) p[j] = pack2(rbf(v[8 * i + 2 * j] - mean) * rstd, rbf(v[8 * i + 2 * j + 1] - mean) * rstd);
        const size_t o = tile + i * 1024 + ((i & 1) ? d1 : d0);
        *(u32x4*)(H2 + o) = p;
        *(u32x4*)(X1 + o) = x1p[i];
      }
    }
  }
}

DI void dma_seg(const char* srcm, unsigned lds_dst, unsigned v0, unsigned v1, unsigned v2, unsigned v3) {
  unsigned keep;
  asm volatile(
      "s_mov_b32 %0, m0\n\t"
      "s_mov_b32 m0, %2\n\t"
      "s_nop 0\n\t"
      "global_load_lds_dwordx4 %3, %1 offset:0\n\t"
      "global_load_lds_dwordx4 %3, %1 offset:1024\n\t"
      "global_load_lds_dwordx4 %4, %1 offset:2048\n\t"
      "global_load_lds_dwordx4 %4, %1 offset:3072\n\t"
      "s_add_u32 m0, m0, 4096\n\t"
      "s_nop 0\n\t"
      "global_load_lds_dwordx4 %5, %1 offset:0\n\t"
      "global_load_lds_dwordx4 %5, %1 offset:1024\n\t"
      "global_load_lds_dwordx4 %6, %1 offset:2048\n\t"
      "global_load_lds_dwordx4 %6, %1 offset:3072\n\t"
      "s_mov_b32 m0, %0"
      : "=&s"(keep) : "s"(srcm), "s"(lds_dst), "v"(v0), "v"(v1), "v"(v2), "v"(v3) : "memory", "scc");
}
DI void wait_vm(int younger_segs) {
  if (younger_segs >= 4) asm volatile("s_waitcnt vmcnt(32)" ::: "memory");
  else if (younger_segs == 3) asm volatile("s_waitcnt vmcnt(24)" ::: "memory");
  else if (younger_segs == 2) asm volatile("s_waitcnt vmcnt(16)" ::: "memory");
  else if (younger_segs == 1) asm volatile("s_waitcnt vmcnt(8)" ::: "memory");
  else asm volatile("s_waitcnt vmcnt(0)" ::: "memory");
}

__global__ __launch_bounds__(256, 1) void k2_spatial(const char* __restrict__ WA, const char* __restrict__ WB,
                                                     const char* __restrict__ H2, const char* __restrict__ X1,
                                                     unsigned short* __restrict__ out) {
  __shared__ __attribute__((aligned(16))) char lds[163840];
  const int t = blockIdx.x & 7, qt = blockIdx.x >> 3;
  if (t == 7) return;
  const int tid = threadIdx.x, w = __builtin_amdgcn_readfirstlane(tid >> 6), l = tid & 63, r = l & 31, h = l >> 5;
  const unsigned lds0 = (unsigned)(size_t)lds;
  const unsigned v0 = l * 16 + (0 ^ w) * 2048 + 2048, v1 = l * 16 + (1 ^ w) * 2048;
  const unsigned v2 = l * 16 + (2 ^ w) * 2048 + 2048, v3 = l * 16 + (3 ^ w) * 2048;
  const char* keysm = H2 + (size_t)t * NTILE * TILEB - 2048;
  const int nt = (w < 3) ? 6 : 5, G = 3 * nt;
  char* ring = lds + w * 40960;
  const unsigned ring0 = lds0 + w * 40960;
  const int so0 = slot_off(0, h, r), so1 = slot_off(1, h, r);
  if (w < 3) dma_seg(keysm + (size_t)qt * TILEB + w * 8192, ring0 + 3 * 8192, v0, v1, v2, v3);
#pragma unroll
  for (int g = 0; g < 3; ++g) dma_seg(keysm + (size_t)w * TILEB + g * 8192, ring0 + g * 8192, v0, v1, v2, v3);
  asm volatile("s_waitcnt vmcnt(24)\n\ts_barrier" ::: "memory");
  const char* wa = WA + (size_t)w * 72 * 1024 + l * 16;
  const char* wb = WB + (size_t)w * 72 * 1024 + l * 16;
  bf16x8 qf[24];
  {
    f32x16 acc[3] = {};
#pragma unroll
    for (int ks = 0; ks < 24; ++ks) {
      const int sg = ks >> 3, cu = (ks >> 1) & 3;
      const bf16x8 hf = *(const bf16x8*)(lds + sg * 40960 + 3 * 8192 + ((cu ^ sg) * 2 + (ks & 1)) * 1024 + ((ks & 1) ? so1 : so0));
#pragma unroll
      for (int i = 0; i < 3; ++i) acc[i] = MFMA(*(const bf16x8*)(wa + (ks * 3 + i) * 1024), hf, acc[i]);
    }
#pragma unroll
    for (int i = 0; i < 3; ++i)
#pragma unroll
      for (int s = 0; s < 2; ++s) *(bf16x8*)(ring + 4 * 8192 + (2 * i + s) * 1024 + l * 16) = pack8(acc[i], s);
    lds_barrier();
#pragma unroll
    for (int u = 0; u < 12; ++u) {
      const int dt = (u & ~3) + ((u & 3) ^ w);
      const int ow = dt / 3;
      const char* p = lds + ow * 40960 + 4 * 8192 + (dt - 3 * ow) * 2048 + l * 16;
      qf[2 * u] = *(const bf16x8*)p; qf[2 * u + 1] = *(const bf16x8*)(p + 1024);
    }
    lds_barrier();
  }
  dma_seg(keysm + (size_t)(w + 4) * TILEB, ring0 + 3 * 8192, v0, v1, v2, v3);
  dma_seg(keysm + (size_t)(w + 4) * TILEB + 8192, ring0 + 4 * 8192, v0, v1, v2, v3);

  const int blk = (l >> 4) & 1, q4 = (l & 15) >> 2, p4 = l & 3, hh = p4 & 1;
  const int xr4 = 4 * (2 * blk + hh);
  const int to0 = blk * 1024 + (hh * 32 + ((4 * h + q4) ^ xr4)) * 16 + 8 * (p4 >> 1);
  const int to1 = blk * 1024 + (hh * 32 + ((8 + 4 * h + q4) ^ xr4)) * 16 + 8 * (p4 >> 1);

  f32x16 o[12];
#pragma unroll
  for (int c = 0; c < 12; ++c) o[c] = f32x16{};
  float mrun = -3.0e38f, lsum = 0.f;
  int slot = 0;
  for (int jj = 0; jj < nt; ++jj) {
    const int s0 = slot, s1 = (slot + 1 >= 5) ? slot - 4 : slot + 1, s2 = (slot + 2 >= 5) ? slot - 3 : slot + 2;
    const int sb[3] = {s0 * 8192, s1 * 8192, s2 * 8192};
    f32x16 sacc = {};
#pragma unroll
    for (int sg = 0; sg < 3; ++sg) {
      wait_vm(min(4 - sg, G - 1 - (3 * jj + sg)));
      const char* base = ring + sb[sg];
#pragma unroll
      for (int k = 0; k < 8; ++k)
        sacc = MFMA(*(const bf16x8*)(base + k * 1024 + ((k & 1) ? so1 : so0)), qf[8 * sg + k], sacc);
    }
    if (4 * jj + w == NTILE - 1) {
#pragma unroll
      for (int i = 0; i < 16; ++i) {
        const int row = (i & 3) + 8 * (i >> 2) + 4 * h;
        if (row >= NP - 32 * (NTILE - 1)) sacc[i] = -3.0e38f;
      }
    }
    float mx = sacc[0];
#pragma unroll
    for (int i = 1; i < 16; ++i) mx = fmaxf(mx, sacc[i]);
    mx = fmaxf(mx, xhalf(mx));
    if (__any(mx > mrun + 8.f)) {
      const float mn = fmaxf(mrun, mx);
      const float f = __builtin_amdgcn_exp2f(mrun - mn);
      mrun = mn; lsum *= f;
#pragma unroll
      for (int c = 0; c < 12; ++c)
#pragma unroll
        for (int i = 0; i < 16; ++i) {
          float tmp;
          asm volatile("v_accvgpr_read_b32 %1, %0\n\tv_mul_f32 %1, %1, %2\n\tv_accvgpr_write_b32 %0, %1" : "+a"(o[c][i]), "=&v"(tmp) : "v"(f));
        }
      asm volatile("s_nop 4");
    }
#pragma unroll
    for (int i = 0; i < 16; ++i) { sacc[i] = __builtin_amdgcn_exp2f(sacc[i] - mrun); lsum += sacc[i]; }
    const bf16x8 pf0 = pack8(sacc, 0), pf1 = pack8(sacc, 1);
#pragma unroll
    for (int sg = 0; sg < 3; ++sg) {
      const char* b0 = ring + sb[sg] + to0;
      const char* b1 = ring + sb[sg] + to1;
#pragma unroll
      for (int c = 0; c < 4; ++c) {
        o[4 * sg + c] = MFMA(tr_frag(b0 + c * 2048, b1 + c * 2048), pf0, o[4 * sg + c]);
        o[4 * sg + c] = MFMA(tr_frag(b0 + c * 2048 + 256, b1 + c * 2048 + 256), pf1, o[4 * sg + c]);
      }
      const int gn = 3 * jj + 5 + sg;
      if (gn < G) {
        asm volatile("s_waitcnt lgkmcnt(0)" ::: "memory");
        const int tj = gn / 3, ts = gn - 3 * tj;
        dma_seg(keysm + (size_t)(4 * tj + w) * TILEB + ts * 8192, ring0 + sb[sg], v0, v1, v2, v3);
      }
    }
    slot = (slot + 3 >= 5) ? slot - 2 : slot + 3;
  }
  lsum += xhalf(lsum);
  lds_barrier();
  f32x2* ml = (f32x2*)(lds + 147456);
  if (h == 0) ml[w * 32 + r] = f32x2{mrun, lsum};
  lds_barrier();
  float fs;
  {
    const f32x2 a = ml[r], b = ml[32 + r], c = ml[64 + r], d = ml[96 + r];
    const float m = fmaxf(fmaxf(a[0], b[0]), fmaxf(c[0], d[0]));
    const float L = a[1] * __builtin_amdgcn_exp2f(a[0] - m) + b[1] * __builtin_amdgcn_exp2f(b[0] - m) +
                    c[1] * __builtin_amdgcn_exp2f(c[0] - m) + d[1] * __builtin_amdgcn_exp2f(d[0] - m);
    fs = __builtin_amdgcn_exp2f(mrun - m) / L;
  }
#pragma unroll
  for (int cc = 1; cc < 4; ++cc) {
    char* dst = lds + (((w ^ cc) * 3 + (cc - 1)) * 3) * 4096 + l * 16;
#pragma unroll
    for (int sg = 0; sg < 3; ++sg)
#pragma unroll
      for (int g = 0; g < 4; ++g)
        *(f32x4*)(dst + sg * 4096 + g * 1024) = f32x4{o[4 * sg + cc][4 * g] * fs, o[4 * sg + cc][4 * g + 1] * fs,
                                                      o[4 * sg + cc][4 * g + 2] * fs, o[4 * sg + cc][4 * g + 3] * fs};
  }
  lds_barrier();
  f32x16 own[3];
#pragma unroll
  for (int sg = 0; sg < 3; ++sg)
#pragma unroll
    for (int i = 0; i < 16; ++i) own[sg][i] = o[4 * sg][i] * fs;
  {
    const char* src = lds + w * 9 * 4096 + l * 16;
#pragma unroll
    for (int s = 0; s < 3; ++s)
#pragma unroll
      for (int sg = 0; sg < 3; ++sg)
#pragma unroll
        for (int g = 0; g < 4; ++g) {
          const f32x4 a = *(const f32x4*)(src + (s * 3 + sg) * 4096 + g * 1024);
          own[sg][4 * g] += a[0]; own[sg][4 * g + 1] += a[1]; own[sg][4 * g + 2] += a[2]; own[sg][4 * g + 3] += a[3];
        }
  }
  lds_barrier();
#pragma unroll
  for (int sg = 0; sg < 3; ++sg)
#pragma unroll
    for (int s = 0; s < 2; ++s) *(bf16x8*)(lds + (2 * (4 * sg + w) + s) * 1024 + l * 16) = pack8(own[sg], s);
  lds_barrier();
  f32x16 acc[3] = {};
#pragma unroll
  for (int ks = 0; ks < 24; ++ks) {
    const bf16x8 of = *(const bf16x8*)(lds + ks * 1024 + l * 16);
#pragma unroll
    for (int i = 0; i < 3; ++i) acc[i] = MFMA(*(const bf16x8*)(wb + (ks * 3 + i) * 1024), of, acc[i]);
  }
  const int n = 32 * qt + r;
  if (n < NP) {
    const char* x1 = X1 + ((size_t)t * NTILE + qt) * TILEB + (size_t)(6 * w) * 1024;
    unsigned short* orow = out + ((size_t)t * NP + n) * CH + 96 * w + 8 * h;
#pragma unroll
    for (int i = 0; i < 6; ++i) {
      const u32x4 xv = *(const u32x4*)(x1 + i * 1024 + ((i & 1) ? so1 : so0));
      u32x4 p;
#pragma unroll
      for (int j = 0; j < 4; ++j)
        p[j] = pack2(bflo(xv[j]) + rbf(acc[i >> 1][8 * (i & 1) + 2 * j]), bfhi(xv[j]) + rbf(acc[i >> 1][8 * (i & 1) + 2 * j + 1]));
      *(u32x4*)(orow + 16 * i) = p;
    }
  }
}

extern "C" void kernel_launch(void* const* d_in, const int* in_sizes, int n_in,
                              void* d_out, int out_size, void* d_ws, size_t ws_size,
                              hipStream_t stream) {
  const unsigned short* x = (const unsigned short*)d_in[0];
  const unsigned short* wqkv_t = (const unsigned short*)d_in[1];
  const unsigned short* wo_t = (const unsigned short*)d_in[2];
  const unsigned short* wqkv_s = (const unsigned short*)d_in[3];
  const unsigned short* wo_s = (const unsigned short*)d_in[4];
  char* ws = (char*)d_ws;
  k0_fold<<<144, 256, 0, stream>>>(wqkv_t, wo_t, wqkv_s, wo_s, ws);
  k1_temporal<<<184, 256, 0, stream>>>(x, ws + OFF_WA_T, ws + OFF_WB_T, ws + OFF_H2, ws + OFF_X1);
  k2_spatial<<<184, 256, 0, stream>>>(ws + OFF_WA_S, ws + OFF_WB_S, ws + OFF_H2, ws + OFF_X1, (unsigned short*)d_out);
}
```

```cpp
#define MIMIC 1
#include <hip/hip_runtime.h>
#include <hip/hip_bf16.h>

using bf16x8 = __attribute__((ext_vector_type(8))) __bf16;
using bf16x2 = __attribute__((ext_vector_type(2))) __bf16;
using s16x4  = __attribute__((ext_vector_type(4))) short;
using s16x8  = __attribute__((ext_vector_type(8))) short;
using f32x16 = __attribute__((ext_vector_type(16))) float;
using f32x4  = __attribute__((ext_vector_type(4))) float;
using f32x2  = __attribute__((ext_vector_type(2))) float;
using u32x4  = __attribute__((ext_vector_type(4))) unsigned;
typedef __attribute__((address_space(3))) s16x4 lds_s16x4;

#define DI __device__ __forceinline__
#define MFMA(a, b, c) __builtin_amdgcn_mfma_f32_32x32x16_bf16((a), (b), (c), 0, 0, 0)

constexpr int TF = 7, NP = 729, CH = 384;
constexpr int NTILE = 23;
constexpr int WBYTES = 12 * 24 * 1024;
constexpr int TILEB = 24 * 1024;
constexpr size_t OFF_WA_T = 0, OFF_WB_T = WBYTES, OFF_WA_S = 2 * WBYTES, OFF_WB_S = 3 * WBYTES;
constexpr size_t OFF_H2 = 4 * (size_t)WBYTES;
constexpr size_t OFF_X1 = OFF_H2 + (size_t)TF * NTILE * TILEB;

DI int perm23(int r) { return (r & 0x13) | ((r & 4) << 1) | ((r & 8) >> 1); }
DI int slot_off(int ksodd, int h, int r) { return (h * 32 + (r ^ (4 * (2 * ksodd + h)))) * 16; }

DI unsigned pack2(float a, float b) {
  f32x2 p = {a, b};
  return __builtin_bit_cast(unsigned, __builtin_convertvector(p, bf16x2));
}
DI bf16x8 pack8(const f32x16& x, int s) {
  u32x4 p;
  p[0] = pack2(x[8 * s + 0], x[8 * s + 1]); p[1] = pack2(x[8 * s + 2], x[8 * s + 3]);
  p[2] = pack2(x[8 * s + 4], x[8 * s + 5]); p[3] = pack2(x[8 * s + 6], x[8 * s + 7]);
  return __builtin_bit_cast(bf16x8, p);
}
DI float bflo(unsigned u) { return __builtin_bit_cast(float, u << 16); }
DI float bfhi(unsigned u) { return __builtin_bit_cast(float, u & 0xffff0000u); }
DI float rbf(float x) { return bflo(pack2(x, 0.f) & 0xffffu); }
DI float rb_row(float x) { return (MIMIC & 1) ? rbf(x) : x; }
DI float rb_elm(float x) { return (MIMIC & 2) ? rbf(x) : x; }
DI float rb_acc(float x) { return (MIMIC & 4) ? rbf(x) : x; }
DI float xsum(float v) {
  const unsigned u = __builtin_bit_cast(unsigned, v);
  const auto r = __builtin_amdgcn_permlane32_swap(u, u, false, false);
  return __builtin_bit_cast(float, (unsigned)r[0]) + __builtin_bit_cast(float, (unsigned)r[1]);
}
DI float xmax(float v) {
  const unsigned u = __builtin_bit_cast(unsigned, v);
  const auto r = __builtin_amdgcn_permlane32_swap(u, u, false, false);
  return fmaxf(__builtin_bit_cast(float, (unsigned)r[0]), __builtin_bit_cast(float, (unsigned)r[1]));
}
DI void lds_barrier() { asm volatile("s_waitcnt lgkmcnt(0)\n\ts_barrier" ::: "memory"); }
#define SB() __builtin_amdgcn_sched_barrier(0)
#define STDECL
#define STAMP(i)
#define STOUT(buf)
template <int I> struct IC { static constexpr int v = I; };
template <int B, int E, class F> DI void sfor(F&& f) { if constexpr (B < E) { f(IC<B>{}); sfor<B + 1, E>(f); } }
template <int OFF> DI void gload_a(bf16x8& dst, unsigned voff, const char* sbase) {
  asm volatile("global_load_dwordx4 %0, %1, %2 offset:%3" : "=a"(dst) : "v"(voff), "s"(sbase), "i"(OFF));
}
template <int OFF> DI void gload_v(bf16x8& dst, unsigned voff, const char* sbase) {
  asm volatile("global_load_dwordx4 %0, %1, %2 offset:%3" : "=v"(dst) : "v"(voff), "s"(sbase), "i"(OFF));
}
template <int OFF> DI void gload_p(u32x4& dst, const void* p) {
  asm volatile("global_load_dwordx4 %0, %1, off offset:%2" : "=v"(dst) : "v"(p), "i"(OFF));
}
template <int IDX, bool AG> DI void wload(bf16x8& dst, unsigned voff, const char* base4k) {
  if constexpr (AG) gload_a<(IDX % 8) * 1024 - 4096>(dst, voff, base4k + (IDX / 8) * 8192);
  else gload_v<(IDX % 8) * 1024 - 4096>(dst, voff, base4k + (IDX / 8) * 8192);
}
template <int N, bool AG> DI void wwait(bf16x8& a) {
  constexpr int M = (N > 63) ? 63 : N;
  if constexpr (AG) asm volatile("s_waitcnt vmcnt(%1)" : "+a"(a) : "i"(M));
  else asm volatile("s_waitcnt vmcnt(%1)" : "+v"(a) : "i"(M));
}
template <bool AG> DI void wtie(bf16x8& a) {
  if constexpr (AG) asm volatile("" : "+a"(a)); else asm volatile("" : "+v"(a));
}
template <int N> DI void wait6(u32x4 (&x)[6]) {
  asm volatile("s_waitcnt vmcnt(%6)" : "+v"(x[0]), "+v"(x[1]), "+v"(x[2]), "+v"(x[3]), "+v"(x[4]), "+v"(x[5]) : "i"(N));
}

DI bf16x8 tr_frag(const char* p0, const char* p1) {
  s16x4 lo = __builtin_amdgcn_ds_read_tr16_b64_v4i16((lds_s16x4*)p0);
  s16x4 hi = __builtin_amdgcn_ds_read_tr16_b64_v4i16((lds_s16x4*)p1);
  s16x8 v = __builtin_shufflevector(lo, hi, 0, 1, 2, 3, 4, 5, 6, 7);
  return __builtin_bit_cast(bf16x8, v);
}

__global__ __launch_bounds__(256) void k0_fold(const unsigned short* __restrict__ wqkv_t, const unsigned short* __restrict__ wo_t,
                                               const unsigned short* __restrict__ wqkv_s, const unsigned short* __restrict__ wo_s,
                                               char* __restrict__ ws) {
  constexpr int RS = 784;
  __shared__ __attribute__((aligned(16))) char lds[2 * 64 * RS];
  char* la = lds;
  char* lb = lds + 64 * RS;
  const int b = blockIdx.x, prod = b / 36, rem = b % 36, nb = rem / 6, cb = rem % 6;
  const int tid = threadIdx.x, w = tid >> 6, l = tid & 63, r = l & 31, h = l >> 5;
  const unsigned short* wqkv = (prod < 2) ? wqkv_t : wqkv_s;
  const unsigned short* wo = (prod < 2) ? wo_t : wo_s;
  const bool isB = prod & 1;
  u32x4 ra[12], rb[12];
  const unsigned short* asrc = wqkv + (size_t)(64 * cb) * 1152 + (isB ? 768 : 0);
#pragma unroll
  for (int i = 0; i < 12; ++i) {
    const int id = tid + 256 * i, row = id / 48, ch = id - 48 * row;
    ra[i] = *(const u32x4*)(asrc + (size_t)row * 1152 + 8 * ch);
  }
  if (!isB) {
    const unsigned short* bsrc = wqkv + (size_t)(64 * nb) * 1152 + 384;
#pragma unroll
    for (int i = 0; i < 12; ++i) {
      const int id = tid + 256 * i, row = id / 48, ch = id - 48 * row;
      rb[i] = *(const u32x4*)(bsrc + (size_t)row * 1152 + 8 * ch);
    }
  } else {
#pragma unroll
    for (int i = 0; i < 12; ++i) {
      const int id = tid + 256 * i;
      rb[i] = *(const u32x4*)(wo + (size_t)(id >> 3) * 384 + 64 * nb + 8 * (id & 7));
    }
  }
  SB();
#pragma unroll
  for (int i = 0; i < 12; ++i) {
    const int id = tid + 256 * i, row = id / 48, ch = id - 48 * row;
    *(u32x4*)(la + row * RS + 16 * ch) = ra[i];
    *(u32x4*)(lb + (isB ? id * 16 : row * RS + 16 * ch)) = rb[i];
  }
  __syncthreads();
  const int ct = w & 1, nt = w >> 1;
  const char* ap = la + (32 * ct + perm23(r)) * RS + 16 * h;
  f32x16 acc = {};
  if (!isB) {
    const char* bp = lb + (32 * nt + perm23(r)) * RS + 16 * h;
#pragma unroll
    for (int ks = 0; ks < 24; ++ks) acc = MFMA(*(const bf16x8*)(ap + 32 * ks), *(const bf16x8*)(bp + 32 * ks), acc);
    const float sc = 0.05103103630798288f * 1.4426950408889634f;
#pragma unroll
    for (int i = 0; i < 16; ++i) acc[i] *= sc;
  } else {
    const int blk = (l >> 4) & 1, q = (l & 15) >> 2, p = l & 3;
    const char* tb = lb + (8 * h + q) * 128 + (32 * nt + 16 * blk + 8 * (p & 1) + 4 * (p >> 1)) * 2;
#pragma unroll
    for (int ks = 0; ks < 24; ++ks) acc = MFMA(*(const bf16x8*)(ap + 32 * ks), tr_frag(tb + ks * 2048, tb + ks * 2048 + 512), acc);
  }
  char* dst = ws + (size_t)prod * WBYTES;
  const int ntile = 2 * nb + nt, ctile = 2 * cb + ct;
#pragma unroll
  for (int s = 0; s < 2; ++s) {
    const int ks = 2 * ctile + s;
    const int idx = (prod < 2) ? ntile * 24 + ks : ((ntile / 3) * 24 + ks) * 3 + ntile % 3;
    *(bf16x8*)(dst + (size_t)idx * 1024 + l * 16) = pack8(acc, s);
  }
}

template <int IMM> DI void dma1(const char* srcm, unsigned m0v, unsigned voff) {
  asm volatile("s_mov_b32 m0, %1\n\ts_nop 0\n\tglobal_load_lds_dwordx4 %2, %0 offset:%3"
               :: "s"(srcm), "s"(m0v), "v"(voff), "i"(IMM) : "memory");
}
template <int N> DI void wait_vm() { asm volatile("s_waitcnt vmcnt(%0)" :: "i"(N) : "memory"); }

DI void dma1p(const void* p, unsigned m0v) {
  asm volatile("s_mov_b32 m0, %1\n\ts_nop 0\n\tglobal_load_lds_dwordx4 %0, off" :: "v"(p), "s"(m0v) : "memory");
}

DI void partial_stats(const float (&v)[16], float& mean_w, float& m2_w) {
  float s = 0.f;
#pragma unroll
  for (int i = 0; i < 16; ++i) s += v[i];
  s = xsum(s);
  mean_w = s * (1.f / 32.f);
  float q = 0.f;
#pragma unroll
  for (int i = 0; i < 16; ++i) { const float d = v[i] - mean_w; q += d * d; }
  m2_w = xsum(q);
}
DI void combine_stats(const char* st, int r, float& mean, float& rstd) {
  f32x4 a[6];
#pragma unroll
  for (int i = 0; i < 6; ++i) a[i] = *(const f32x4*)(st + r * 112 + 16 * i);
  float m = 0.f, m2 = 0.f;
#pragma unroll
  for (int i = 0; i < 6; ++i) { m += a[i][0] + a[i][2]; m2 += a[i][1] + a[i][3]; }
  m *= (1.f / 12.f);
  float d2 = 0.f;
#pragma unroll
  for (int i = 0; i < 6; ++i) { const float d0 = a[i][0] - m, d1 = a[i][2] - m; d2 += d0 * d0 + d1 * d1; }
  const float var = (m2 + 32.f * d2) * (1.f / 384.f);
  mean = rb_row(m);
  rstd = 1.f / rb_row(sqrtf(rb_row(rb_row(var) + 1e-5f)));
}
template <int N> DI void wait2(u32x4 (&x)[2]) { asm volatile("s_waitcnt vmcnt(%2)" : "+v"(x[0]), "+v"(x[1]) : "i"(N)); }

__global__ __launch_bounds__(768, 3) void k1_temporal(const unsigned short* __restrict__ x, const char* __restrict__ WA,
                                                      const char* __restrict__ WB, char* __restrict__ H2, char* __restrict__ X1) {
  __shared__ __attribute__((aligned(16))) char lds[2 * TILEB + 2048 + 2 * 3584];
  char* himg = lds;
  char* qimg = lds + TILEB;
  char* amix = lds + 2 * TILEB;
  char* st1 = lds + 2 * TILEB + 2048;
  char* st2 = st1 + 3584;
  const int tid = threadIdx.x, w = __builtin_amdgcn_readfirstlane(tid >> 6), l = tid & 63, r = l & 31, h = l >> 5;
  const int pos = r & 3, t = r >> 2, n = 4 * blockIdx.x + pos;
  const bool valid = (r < 28) && (n < NP);
  STDECL; STAMP(0);
  bf16x8 wf[48];
  const unsigned voff = l * 16;
  const char* wa4 = WA + (size_t)w * 24 * 1024 + 4096;
  const char* wb4 = WB + (size_t)w * 24 * 1024 + 4096;
  auto wissue = [&](auto I) {
    constexpr int f = I.v;
    if constexpr (f < 24) wload<f, false>(wf[f], voff, wa4);
    else if constexpr (f < 48) wload<f - 24, false>(wf[f], voff, wb4);
  };
  const int so0 = slot_off(0, h, r), so1 = slot_off(1, h, r);
  u32x4 xr[2];
  {
#pragma unroll
    for (int i = 0; i < 2; ++i) {
      const int id = (2 * w + i) * 64 + l, row0 = id / 48, ch = id - 48 * row0;
      const int row = (row0 < 28) ? row0 : 27, nn = min(4 * (int)blockIdx.x + (row & 3), NP - 1);
      dma1p(x + ((size_t)(row >> 2) * NP + nn) * CH + 8 * ch, (unsigned)(size_t)qimg + (2 * w + i) * 1024);
    }
    sfor<0, 12>(wissue);
    wait_vm<12>();
    asm volatile("s_barrier" ::: "memory");
    STAMP(1);
    const char* xp = qimg + (r < 28 ? r : 0) * 768 + 64 * w + 16 * h;
    xr[0] = *(const u32x4*)xp; xr[1] = *(const u32x4*)(xp + 32);
#pragma unroll
    for (int i = 0; i < 2; ++i) if (!valid) xr[i] = u32x4{0, 0, 0, 0};
    float v[16];
#pragma unroll
    for (int i = 0; i < 2; ++i)
#pragma unroll
      for (int j = 0; j < 4; ++j) { v[8 * i + 2 * j] = bflo(xr[i][j]); v[8 * i + 2 * j + 1] = bfhi(xr[i][j]); }
    float mw, m2;
    partial_stats(v, mw, m2);
    if (h == 0) *(f32x2*)(st1 + r * 112 + w * 8) = f32x2{mw, m2};
    SB();
    sfor<12, 18>(wissue);
    lds_barrier();
    float mean, rstd;
    combine_stats(st1, r, mean, rstd);
#pragma unroll
    for (int i = 0; i < 2; ++i) {
      u32x4 p;
#pragma unroll
      for (int j = 0; j < 4; ++j) p[j] = pack2(rb_elm(v[8 * i + 2 * j] - mean) * rstd, rb_elm(v[8 * i + 2 * j + 1] - mean) * rstd);
      *(u32x4*)(himg + (2 * w + i) * 1024 + (i ? so1 : so0)) = p;
    }
    SB();
    sfor<18, 24>(wissue);
    lds_barrier();
  }
  STAMP(2);
  f32x16 acc = {};
  {
    bf16x8 hf[24];
    hf[0] = *(const bf16x8*)(himg + so0); hf[1] = *(const bf16x8*)(himg + 1024 + so1);
    SB();
    sfor<0, 24>([&](auto I) {
      constexpr int ks = I.v;
      if constexpr (ks + 2 < 24) hf[ks + 2] = *(const bf16x8*)(himg + (ks + 2) * 1024 + ((ks & 1) ? so1 : so0));
      if constexpr (ks & 1) wissue(IC<24 + ks / 2>{});
      wwait<23 - ks + (ks + 1) / 2, false>(wf[ks]);
      acc = MFMA(wf[ks], hf[ks], acc);
      SB();
    });
  }
  STAMP(3);
  *(bf16x8*)(qimg + (2 * w) * 1024 + l * 16) = pack8(acc, 0);
  *(bf16x8*)(qimg + (2 * w + 1) * 1024 + l * 16) = pack8(acc, 1);
  lds_barrier();
  if (w == 0) {
    f32x16 sc;
#pragma unroll
    for (int i = 0; i < 16; ++i) {
      const int row = (i & 3) + 8 * (i >> 2) + 4 * h;
      sc[i] = ((i & 3) == pos && row < 28) ? 0.f : -3.0e38f;
    }
    bf16x8 ha[24], qb[24];
    sfor<0, 5>([&](auto I) {
      ha[I.v] = *(const bf16x8*)(himg + I.v * 1024 + ((I.v & 1) ? so1 : so0));
      qb[I.v] = *(const bf16x8*)(qimg + I.v * 1024 + l * 16);
    });
    SB();
    sfor<0, 24>([&](auto I) {
      constexpr int ks = I.v;
      if constexpr (ks + 5 < 24) {
        ha[ks + 5] = *(const bf16x8*)(himg + (ks + 5) * 1024 + (((ks + 5) & 1) ? so1 : so0));
        qb[ks + 5] = *(const bf16x8*)(qimg + (ks + 5) * 1024 + l * 16);
      }
      sc = MFMA(ha[ks], qb[ks], sc);
      SB();
    });
    float mx = sc[0];
#pragma unroll
    for (int i = 1; i < 16; ++i) mx = fmaxf(mx, sc[i]);
    mx = xmax(mx);
    float sum = 0.f;
#pragma unroll
    for (int i = 0; i < 16; ++i) { sc[i] = __builtin_amdgcn_exp2f(sc[i] - mx); sum += sc[i]; }
    const float inv = 1.f / xsum(sum);
#pragma unroll
    for (int i = 0; i < 16; ++i) sc[i] *= inv;
    *(bf16x8*)(amix + l * 16) = pack8(sc, 0);
    *(bf16x8*)(amix + 1024 + l * 16) = pack8(sc, 1);
    lds_barrier();
  }
  SB();
  sfor<36, 48>(wissue);
  if (w != 0) lds_barrier();
  STAMP(4);
  {
    const int blk = (l >> 4) & 1, q = (l & 15) >> 2, p = l & 3, hh = p & 1;
    const int xr4 = 4 * (2 * blk + hh);
    const char* tb0 = himg + w * 2048 + blk * 1024 + (hh * 32 + ((4 * h + q) ^ xr4)) * 16 + 8 * (p >> 1);
    const char* tb1 = himg + w * 2048 + blk * 1024 + (hh * 32 + ((8 + 4 * h + q) ^ xr4)) * 16 + 8 * (p >> 1);
    f32x16 a = {};
    a = MFMA(tr_frag(tb0, tb1), *(const bf16x8*)(amix + l * 16), a);
    a = MFMA(tr_frag(tb0 + 256, tb1 + 256), *(const bf16x8*)(amix + 1024 + l * 16), a);
    *(bf16x8*)(qimg + (2 * w) * 1024 + l * 16) = pack8(a, 0);
    *(bf16x8*)(qimg + (2 * w + 1) * 1024 + l * 16) = pack8(a, 1);
  }
  lds_barrier();
  STAMP(5);
  acc = f32x16{};
  {
    bf16x8 hf[24];
    hf[0] = *(const bf16x8*)(qimg + l * 16); hf[1] = *(const bf16x8*)(qimg + 1024 + l * 16);
    SB();
    sfor<0, 24>([&](auto I) {
      constexpr int ks = I.v;
      if constexpr (ks + 2 < 24) hf[ks + 2] = *(const bf16x8*)(qimg + (ks + 2) * 1024 + l * 16);
      wwait<23 - ks, false>(wf[24 + ks]);
      acc = MFMA(wf[24 + ks], hf[ks], acc);
      SB();
    });
  }
  STAMP(6);
  {
    float v[16];
    u32x4 x1p[2];
#pragma unroll
    for (int i = 0; i < 2; ++i)
#pragma unroll
      for (int j = 0; j < 4; ++j) {
        const float a = bflo(xr[i][j]) + rb_acc(acc[8 * i + 2 * j]);
        const float b = bfhi(xr[i][j]) + rb_acc(acc[8 * i + 2 * j + 1]);
        const unsigned u = pack2(a, b);
        x1p[i][j] = u;
        v[8 * i + 2 * j] = bflo(u); v[8 * i + 2 * j + 1] = bfhi(u);
      }
    float mw, m2;
    partial_stats(v, mw, m2);
    if (h == 0) *(f32x2*)(st2 + r * 112 + w * 8) = f32x2{mw, m2};
    const int rr = n & 31;
    const size_t tile = ((size_t)t * NTILE + (n >> 5)) * TILEB + (size_t)(2 * w) * 1024;
    const int d0 = slot_off(0, h, rr), d1 = slot_off(1, h, rr);
    if (r < 28) { *(u32x4*)(X1 + tile + d0) = x1p[0]; *(u32x4*)(X1 + tile + 1024 + d1) = x1p[1]; }
    lds_barrier();
    float mean, rstd;
    combine_stats(st2, r, mean, rstd);
    if (r < 28) {
#pragma unroll
      for (int i = 0; i < 2; ++i) {
        u32x4 p;
#pragma unroll
        for (int j = 0; j < 4; ++j) p[j] = pack2(rb_elm(v[8 * i + 2 * j] - mean) * rstd, rb_elm(v[8 * i + 2 * j + 1] - mean) * rstd);
        *(u32x4*)(H2 + tile + i * 1024 + (i ? d1 : d0)) = p;
      }
    }
  }
  STAMP(7);
  STOUT(X1 + (size_t)TF * NTILE * TILEB);
}

__global__ __launch_bounds__(256, 1) void k2_spatial(const char* __restrict__ WA, const char* __restrict__ WB,
                                                     const char* __restrict__ H2, const char* __restrict__ X1,
                                                     unsigned short* __restrict__ out) {
  __shared__ __attribute__((aligned(16))) char lds[163840];
  const int t = blockIdx.x & 7, qt = blockIdx.x >> 3;
  if (t == 7) return;
  const int tid = threadIdx.x, w = __builtin_amdgcn_readfirstlane(tid >> 6), l = tid & 63, r = l & 31, h = l >> 5;
  const unsigned lds0 = (unsigned)(size_t)lds;
  const unsigned voff = l * 16;
  unsigned vu[4];
  vu[0] = voff + (0 ^ w) * 2048 + 2048; vu[1] = voff + (1 ^ w) * 2048;
  vu[2] = voff + (2 ^ w) * 2048 + 2048; vu[3] = voff + (3 ^ w) * 2048;
  const char* keysm = H2 + (size_t)t * NTILE * TILEB - 2048;
  const int nt = (w < 3) ? 6 : 5;
  char* ring = lds + w * 40960;
  const unsigned ring0 = lds0 + w * 40960;
  const int so0 = slot_off(0, h, r), so1 = slot_off(1, h, r);
  const char* wa4 = WA + (size_t)w * 72 * 1024 + 4096;
  const char* wb4 = WB + (size_t)w * 72 * 1024 + 4096;
  auto dma_frag = [&](auto I, const char* srcm, const unsigned (&m)[3]) {
    constexpr int f = I.v, sg = f >> 3, k = f & 7, cc = k >> 1;
    dma1<(k & 3) * 1024>(srcm + sg * 8192, m[sg] + (cc >= 2 ? 4096 : 0), vu[cc]);
  };

  STDECL; STAMP(0);
  bf16x8 qf[24];
  {
    constexpr int WIN = 30;
    bf16x8 wf[72];
    {
      const char* src = keysm + 2048 + (size_t)qt * TILEB + w * 6144;
      sfor<0, 4>([&](auto I) { dma1<I.v * 1024>(src, ring0 + 4 * 8192, voff); });
      sfor<0, 2>([&](auto I) { dma1<I.v * 1024>(src + 4096, ring0 + 4 * 8192 + 4096, voff); });
    }
    sfor<0, WIN>([&](auto I) { wload<I.v, true>(wf[I.v], voff, wa4); });
    {
      const unsigned m[3] = {ring0, ring0 + 8192, ring0 + 16384};
      sfor<0, 24>([&](auto I) { dma_frag(I, keysm + (size_t)w * TILEB, m); });
    }
    STAMP(1);
    wait_vm<WIN + 24>();
    asm volatile("s_barrier" ::: "memory");
    STAMP(2);
    f32x16 acc[3] = {};
    bf16x8 hf[24];
    auto hread = [&](auto I) {
      constexpr int ks = I.v;
      hf[ks] = *(const bf16x8*)(lds + (ks / 6) * 40960 + 4 * 8192 + (ks % 6) * 1024 + ((ks & 1) ? so1 : so0));
    };
    sfor<0, 2>(hread);
    SB();
    sfor<0, 24>([&](auto I) {
      constexpr int ks = I.v;
      if constexpr (ks + 2 < 24) hread(IC<ks + 2>{});
      sfor<3 * ks + WIN, 3 * ks + WIN + 3>([&](auto J) { if constexpr (J.v < 72) wload<J.v, true>(wf[J.v], voff, wa4); });
      constexpr int issued = ((3 * ks + 3 + WIN < 72) ? 3 * ks + 3 + WIN : 72) + 24;
      constexpr int pos = (3 * ks + 2 < WIN) ? 3 * ks + 2 : 3 * ks + 2 + 24;
      wwait<issued - 1 - pos, true>(wf[3 * ks + 2]); wtie<true>(wf[3 * ks]); wtie<true>(wf[3 * ks + 1]);
#pragma unroll
      for (int i = 0; i < 3; ++i) acc[i] = MFMA(wf[3 * ks + i], hf[ks], acc[i]);
      SB();
    });
    STAMP(3);
    lds_barrier();
#pragma unroll
    for (int i = 0; i < 3; ++i)
#pragma unroll
      for (int s = 0; s < 2; ++s) *(bf16x8*)(ring + 4 * 8192 + (2 * i + s) * 1024 + l * 16) = pack8(acc[i], s);
    lds_barrier();
#pragma unroll
    for (int u = 0; u < 12; ++u) {
      const int dt = (u & ~3) + ((u & 3) ^ w);
      const int ow = dt / 3;
      const char* p = lds + ow * 40960 + 4 * 8192 + (dt - 3 * ow) * 2048 + l * 16;
      qf[2 * u] = *(const bf16x8*)p; qf[2 * u + 1] = *(const bf16x8*)(p + 1024);
    }
    lds_barrier();
  }

  const int blk = (l >> 4) & 1, q4 = (l & 15) >> 2, p4 = l & 3, hh = p4 & 1;
  const int xr4 = 4 * (2 * blk + hh);
  const int to0 = blk * 1024 + (hh * 32 + ((4 * h + q4) ^ xr4)) * 16 + 8 * (p4 >> 1);
  const int to1 = blk * 1024 + (hh * 32 + ((8 + 4 * h + q4) ^ xr4)) * 16 + 8 * (p4 >> 1);

  f32x16 o[12];
#pragma unroll
  for (int c = 0; c < 12; ++c) o[c] = f32x16{};
  float mrun = -3.0e38f, lsum = 0.f;
  bf16x8 w2[72];

  auto period = [&](auto LASTC, int jj, int slot) {
    constexpr bool LAST = LASTC.v;
    constexpr int PF = 3;
    const int s1 = (slot + 1 >= 5) ? slot - 4 : slot + 1, s2 = (slot + 2 >= 5) ? slot - 3 : slot + 2;
    const int s3 = (slot + 3 >= 5) ? slot - 2 : slot + 3, s4 = (slot + 4 >= 5) ? slot - 1 : slot + 4;
    const char* rb[3] = {ring + slot * 8192, ring + s1 * 8192, ring + s2 * 8192};
    const unsigned m[3] = {ring0 + s3 * 8192, ring0 + s4 * 8192, ring0 + slot * 8192};
    const char* nsrc = keysm + (size_t)(4 * (jj + 1) + w) * TILEB;
    auto stream = [&](auto I) {
      if constexpr (LAST) wload<I.v, (I.v < 12)>(w2[I.v], voff, wb4);
      else dma_frag(I, nsrc, m);
    };
    bf16x8 kf[24], tf[24];
    auto kread = [&](auto I) {
      constexpr int f = I.v;
      if constexpr ((f & 1) == 0) wait_vm<23 - (f + 1) + ((f > PF) ? (f - PF) / 2 : 0)>();
      kf[f] = *(const bf16x8*)(rb[f >> 3] + (f & 7) * 1024 + ((f & 1) ? so1 : so0));
    };
    auto tread = [&](auto I) {
      constexpr int p = I.v, u = p >> 1;
      const char* b = rb[u >> 2] + (u & 3) * 2048 + (p & 1) * 256;
      tf[p] = tr_frag(b + to0, b + to1);
    };
    f32x16 sacc = {};
    sfor<0, PF>(kread);
    SB();
    sfor<0, 24>([&](auto I) {
      constexpr int f = I.v;
      if constexpr (f + PF < 24) kread(IC<f + PF>{});
      sacc = MFMA(kf[f], qf[f], sacc);
      if constexpr (f & 1) stream(IC<f / 2>{});
      SB();
    });
    sfor<0, 2>(tread);
    if (4 * jj + w == NTILE - 1) {
#pragma unroll
      for (int i = 0; i < 16; ++i) {
        const int row = (i & 3) + 8 * (i >> 2) + 4 * h;
        if (row >= NP - 32 * (NTILE - 1)) sacc[i] = -3.0e38f;
      }
    }
    float mx = sacc[0];
#pragma unroll
    for (int i = 1; i < 16; ++i) mx = fmaxf(mx, sacc[i]);
    mx = xmax(mx);
    if (__any(mx > mrun + 8.f)) {
      const float mn = fmaxf(mrun, mx);
      const float f = __builtin_amdgcn_exp2f(mrun - mn);
      mrun = mn; lsum *= f;
#pragma unroll
      for (int c = 0; c < 12; ++c)
#pragma unroll
        for (int i = 0; i < 16; ++i) {
          float tmp;
          asm volatile("v_accvgpr_read_b32 %1, %0\n\tv_mul_f32 %1, %1, %2\n\tv_accvgpr_write_b32 %0, %1" : "+a"(o[c][i]), "=&v"(tmp) : "v"(f));
        }
      asm volatile("s_nop 4");
    }
#pragma unroll
    for (int i = 0; i < 16; ++i) { sacc[i] = __builtin_amdgcn_exp2f(sacc[i] - mrun); lsum += sacc[i]; }
    const bf16x8 pf0 = pack8(sacc, 0), pf1 = pack8(sacc, 1);
    SB();
    sfor<0, 24>([&](auto I) {
      constexpr int p = I.v;
      if constexpr (p + 2 < 24) tread(IC<p + 2>{});
      o[p >> 1] = MFMA(tf[p], (p & 1) ? pf1 : pf0, o[p >> 1]);
      if constexpr (p & 1) stream(IC<12 + p / 2>{});
      SB();
    });
  };
  int slot = 0;
  STAMP(4);
  for (int jj = 0; jj < nt - 1; ++jj) {
    period(IC<false>{}, jj, slot);
    slot = (slot + 3 >= 5) ? slot - 2 : slot + 3;
  }
  STAMP(5);
  period(IC<true>{}, nt - 1, slot);
  STAMP(6);
  sfor<24, 42>([&](auto I) { wload<I.v, false>(w2[I.v], voff, wb4); });

  lsum = xsum(lsum);
  lds_barrier();
  f32x2* ml = (f32x2*)(lds + 147456);
  if (h == 0) ml[w * 32 + r] = f32x2{mrun, lsum};
#pragma unroll
  for (int cc = 1; cc < 4; ++cc) {
    char* dst = lds + (((w ^ cc) * 3 + (cc - 1)) * 3) * 4096 + l * 16;
#pragma unroll
    for (int sg = 0; sg < 3; ++sg)
#pragma unroll
      for (int g = 0; g < 4; ++g)
        *(f32x4*)(dst + sg * 4096 + g * 1024) = f32x4{o[4 * sg + cc][4 * g], o[4 * sg + cc][4 * g + 1],
                                                      o[4 * sg + cc][4 * g + 2], o[4 * sg + cc][4 * g + 3]};
  }
  SB();
  sfor<42, 72>([&](auto I) { wload<I.v, true>(w2[I.v], voff, wb4); });
  u32x4 xv[6];
  {
    const char* x1 = X1 + ((size_t)t * NTILE + qt) * TILEB + (size_t)(6 * w) * 1024;
    const char* xa = x1 + so0;
    const char* xb = x1 + so1;
    sfor<0, 6>([&](auto I) { gload_p<(I.v & ~1) * 1024 - 2048 + (I.v & 1) * 1024>(xv[I.v], ((I.v & 1) ? xb : xa) + 2048); });
  }
  lds_barrier();
  bf16x8 opk[6];
  {
    float fs[4];
    {
      const f32x2 a = ml[w * 32 + r], b = ml[(w ^ 1) * 32 + r], c = ml[(w ^ 2) * 32 + r], d = ml[(w ^ 3) * 32 + r];
      const float m = fmaxf(fmaxf(a[0], b[0]), fmaxf(c[0], d[0]));
      fs[0] = __builtin_amdgcn_exp2f(a[0] - m); fs[1] = __builtin_amdgcn_exp2f(b[0] - m);
      fs[2] = __builtin_amdgcn_exp2f(c[0] - m); fs[3] = __builtin_amdgcn_exp2f(d[0] - m);
      const float inv = 1.f / (a[1] * fs[0] + b[1] * fs[1] + c[1] * fs[2] + d[1] * fs[3]);
      fs[0] *= inv; fs[1] *= inv; fs[2] *= inv; fs[3] *= inv;
    }
    const char* src = lds + w * 9 * 4096 + l * 16;
#pragma unroll
    for (int sg = 0; sg < 3; ++sg) {
      f32x16 own;
#pragma unroll
      for (int i = 0; i < 16; ++i) {
        float tmp;
        asm volatile("v_accvgpr_read_b32 %0, %1" : "=v"(tmp) : "a"(o[4 * sg][i]));
        own[i] = tmp * fs[0];
      }
#pragma unroll
      for (int s = 0; s < 3; ++s)
#pragma unroll
        for (int g = 0; g < 4; ++g) {
          const f32x4 a = *(const f32x4*)(src + (s * 3 + sg) * 4096 + g * 1024);
          own[4 * g] += a[0] * fs[s + 1]; own[4 * g + 1] += a[1] * fs[s + 1];
          own[4 * g + 2] += a[2] * fs[s + 1]; own[4 * g + 3] += a[3] * fs[s + 1];
        }
      opk[2 * sg] = pack8(own, 0); opk[2 * sg + 1] = pack8(own, 1);
      asm volatile("" : "+v"(opk[2 * sg]), "+v"(opk[2 * sg + 1]) :: "memory");
    }
  }
  lds_barrier();
#pragma unroll
  for (int sg = 0; sg < 3; ++sg)
#pragma unroll
    for (int s = 0; s < 2; ++s) *(bf16x8*)(lds + (2 * (4 * sg + w) + s) * 1024 + l * 16) = opk[2 * sg + s];
  lds_barrier();
  STAMP(7);
  f32x16 acc[3] = {};
  {
    bf16x8 of[24];
    of[0] = *(const bf16x8*)(lds + l * 16); of[1] = *(const bf16x8*)(lds + 1024 + l * 16);
    SB();
    sfor<0, 24>([&](auto I) {
      constexpr int ks = I.v;
      if constexpr (ks + 2 < 24) of[ks + 2] = *(const bf16x8*)(lds + (ks + 2) * 1024 + l * 16);
      constexpr auto isa = [](int f) { return f < 12 || f >= 42; };
      wwait<72 + 6 - 1 - (3 * ks + 2), isa(3 * ks + 2)>(w2[3 * ks + 2]); wtie<isa(3 * ks)>(w2[3 * ks]); wtie<isa(3 * ks + 1)>(w2[3 * ks + 1]);
#pragma unroll
      for (int i = 0; i < 3; ++i) acc[i] = MFMA(w2[3 * ks + i], of[ks], acc[i]);
      SB();
    });
  }
  STAMP(8);
  wait6<0>(xv);
  const int n = 32 * qt + r;
  if (n < NP) {
    unsigned short* orow = out + ((size_t)t * NP + n) * CH + 96 * w + 8 * h;
#pragma unroll
    for (int i = 0; i < 6; ++i) {
      u32x4 p;
#pragma unroll
      for (int j = 0; j < 4; ++j)
        p[j] = pack2(bflo(xv[i][j]) + rb_acc(acc[i >> 1][8 * (i & 1) + 2 * j]), bfhi(xv[i][j]) + rb_acc(acc[i >> 1][8 * (i & 1) + 2 * j + 1]));
      *(u32x4*)(orow + 16 * i) = p;
    }
  }
  STAMP(9);
  STOUT((char*)X1 + (size_t)TF * NTILE * TILEB + 1048576);
}

extern "C" void kernel_launch(void* const* d_in, const int* in_sizes, int n_in,
                              void* d_out, int out_size, void* d_ws, size_t ws_size,
                              hipStream_t stream) {
  const unsigned short* x = (const unsigned short*)d_in[0];
  const unsigned short* wqkv_t = (const unsigned short*)d_in[1];
  const unsigned short* wo_t = (const unsigned short*)d_in[2];
  const unsigned short* wqkv_s = (const unsigned short*)d_in[3];
  const unsigned short* wo_s = (const unsigned short*)d_in[4];
  char* ws = (char*)d_ws;
  k0_fold<<<144, 256, 0, stream>>>(wqkv_t, wo_t, wqkv_s, wo_s, ws);
  k1_temporal<<<184, 768, 0, stream>>>(x, ws + OFF_WA_T, ws + OFF_WB_T, ws + OFF_H2, ws + OFF_X1);
  k2_spatial<<<184, 256, 0, stream>>>(ws + OFF_WA_S, ws + OFF_WB_S, ws + OFF_H2, ws + OFF_X1, (unsigned short*)d_out);
}
```

```cpp
#include <hip/hip_runtime.h>
#include <hip/hip_bf16.h>

using bf16x8 = __attribute__((ext_vector_type(8))) __bf16;
using bf16x2 = __attribute__((ext_vector_type(2))) __bf16;
using s16x4  = __attribute__((ext_vector_type(4))) short;
using s16x8  = __attribute__((ext_vector_type(8))) short;
using f32x16 = __attribute__((ext_vector_type(16))) float;
using f32x4  = __attribute__((ext_vector_type(4))) float;
using f32x2  = __attribute__((ext_vector_type(2))) float;
using u32x4  = __attribute__((ext_vector_type(4))) unsigned;
typedef __attribute__((address_space(3))) s16x4 lds_s16x4;

#define DI __device__ __forceinline__
#define MFMA(a, b, c) __builtin_amdgcn_mfma_f32_32x32x16_bf16((a), (b), (c), 0, 0, 0)

constexpr int TF = 7, NP = 729, CH = 384;
constexpr int NTILE = 23;
constexpr int WBYTES = 12 * 24 * 1024;
constexpr int TILEB = 24 * 1024;
constexpr size_t OFF_WA_T = 0, OFF_WB_T = WBYTES, OFF_WA_S = 2 * WBYTES, OFF_WB_S = 3 * WBYTES;
constexpr size_t OFF_H2 = 4 * (size_t)WBYTES;
constexpr size_t OFF_X1 = OFF_H2 + (size_t)TF * NTILE * TILEB;

DI int perm23(int r) { return (r & 0x13) | ((r & 4) << 1) | ((r & 8) >> 1); }
DI int slot_off(int ksodd, int h, int r) { return (h * 32 + (r ^ (4 * (2 * ksodd + h)))) * 16; }

DI unsigned pack2(float a, float b) {
  f32x2 p = {a, b};
  return __builtin_bit_cast(unsigned, __builtin_convertvector(p, bf16x2));
}
DI bf16x8 pack8(const f32x16& x, int s) {
  u32x4 p;
  p[0] = pack2(x[8 * s + 0], x[8 * s + 1]); p[1] = pack2(x[8 * s + 2], x[8 * s + 3]);
  p[2] = pack2(x[8 * s + 4], x[8 * s + 5]); p[3] = pack2(x[8 * s + 6], x[8 * s + 7]);
  return __builtin_bit_cast(bf16x8, p);
}
DI float bflo(unsigned u) { return __builtin_bit_cast(float, u << 16); }
DI float bfhi(unsigned u) { return __builtin_bit_cast(float, u & 0xffff0000u); }
DI f32x2 unpk(unsigned u) { return f32x2{bflo(u), bfhi(u)}; }
DI unsigned pk(f32x2 p) { return __builtin_bit_cast(unsigned, __builtin_convertvector(p, bf16x2)); }
DI float xsum(float v) {
  const unsigned u = __builtin_bit_cast(unsigned, v);
  const auto r = __builtin_amdgcn_permlane32_swap(u, u, false, false);
  return __builtin_bit_cast(float, (unsigned)r[0]) + __builtin_bit_cast(float, (unsigned)r[1]);
}
DI float xmax(float v) {
  const unsigned u = __builtin_bit_cast(unsigned, v);
  const auto r = __builtin_amdgcn_permlane32_swap(u, u, false, false);
  return fmaxf(__builtin_bit_cast(float, (unsigned)r[0]), __builtin_bit_cast(float, (unsigned)r[1]));
}
DI void lds_barrier() { asm volatile("s_waitcnt lgkmcnt(0)\n\ts_barrier" ::: "memory"); }
#define SB() __builtin_amdgcn_sched_barrier(0)
#define STDECL
#define STAMP(i)
#define STACC(i, prev)
#define STOUT(buf)
template <int I> struct IC { static constexpr int v = I; };
template <int B, int E, class F> DI void sfor(F&& f) { if constexpr (B < E) { f(IC<B>{}); sfor<B + 1, E>(f); } }
template <int OFF> DI void gload_a(bf16x8& dst, unsigned voff, const char* sbase) {
  asm volatile("global_load_dwordx4 %0, %1, %2 offset:%3" : "=a"(dst) : "v"(voff), "s"(sbase), "i"(OFF));
}
template <int OFF> DI void gload_v(bf16x8& dst, unsigned voff, const char* sbase) {
  asm volatile("global_load_dwordx4 %0, %1, %2 offset:%3" : "=v"(dst) : "v"(voff), "s"(sbase), "i"(OFF));
}
template <int OFF> DI void gload_p(u32x4& dst, const void* p) {
  asm volatile("global_load_dwordx4 %0, %1, off offset:%2" : "=v"(dst) : "v"(p), "i"(OFF));
}
template <int IDX, bool AG> DI void wload(bf16x8& dst, unsigned voff, const char* base4k) {
  if constexpr (AG) gload_a<(IDX % 8) * 1024 - 4096>(dst, voff, base4k + (IDX / 8) * 8192);
  else gload_v<(IDX % 8) * 1024 - 4096>(dst, voff, base4k + (IDX / 8) * 8192);
}
template <int N, bool AG> DI void wwait(bf16x8& a) {
  constexpr int M = (N > 63) ? 63 : N;
  if constexpr (AG) asm volatile("s_waitcnt vmcnt(%1)" : "+a"(a) : "i"(M));
  else asm volatile("s_waitcnt vmcnt(%1)" : "+v"(a) : "i"(M));
}
template <bool AG> DI void wtie(bf16x8& a) {
  if constexpr (AG) asm volatile("" : "+a"(a)); else asm volatile("" : "+v"(a));
}
template <int N> DI void wait6(u32x4 (&x)[6]) {
  asm volatile("s_waitcnt vmcnt(%6)" : "+v"(x[0]), "+v"(x[1]), "+v"(x[2]), "+v"(x[3]), "+v"(x[4]), "+v"(x[5]) : "i"(N));
}

DI bf16x8 tr_frag(const char* p0, const char* p1) {
  s16x4 lo = __builtin_amdgcn_ds_read_tr16_b64_v4i16((lds_s16x4*)p0);
  s16x4 hi = __builtin_amdgcn_ds_read_tr16_b64_v4i16((lds_s16x4*)p1);
  s16x8 v = __builtin_shufflevector(lo, hi, 0, 1, 2, 3, 4, 5, 6, 7);
  return __builtin_bit_cast(bf16x8, v);
}

template <int IMM> DI void dma1(const char* srcm, unsigned m0v, unsigned voff) {
  asm volatile("s_mov_b32 m0, %1\n\ts_nop 0\n\tglobal_load_lds_dwordx4 %2, %0 offset:%3"
               :: "s"(srcm), "s"(m0v), "v"(voff), "i"(IMM) : "memory");
}
template <int N> DI void wait_vm() { asm volatile("s_waitcnt vmcnt(%0)" :: "i"(N) : "memory"); }

DI void gstore(void* p, u32x4 v) {
  asm volatile("global_store_dwordx4 %0, %1, off sc1\n\ts_nop 1" :: "v"(p), "v"(v) : "memory");
}
DI void dma1p(const void* p, unsigned m0v) {
  asm volatile("s_mov_b32 m0, %1\n\ts_nop 0\n\tglobal_load_lds_dwordx4 %0, off" :: "v"(p), "s"(m0v) : "memory");
}

__global__ __launch_bounds__(256) void k0_fold(const unsigned short* __restrict__ wqkv_t, const unsigned short* __restrict__ wo_t,
                                               const unsigned short* __restrict__ wqkv_s, const unsigned short* __restrict__ wo_s,
                                               char* __restrict__ ws) {
  __shared__ __attribute__((aligned(16))) char lds[2 * 49152];
  char* la = lds;
  char* lb = lds + 49152;
  const int b = blockIdx.x, prod = b / 36, rem = b % 36, nb = rem / 6, cb = rem % 6;
  const int tid = threadIdx.x, w = __builtin_amdgcn_readfirstlane(tid >> 6), l = tid & 63, r = l & 31, h = l >> 5;
  const unsigned short* wqkv = (prod < 2) ? wqkv_t : wqkv_s;
  const unsigned short* wo = (prod < 2) ? wo_t : wo_s;
  const bool isB = prod & 1;
  STDECL; STAMP(0);
  {
    const unsigned short* asrc = wqkv + (size_t)(64 * cb + 16 * w) * 1152 + (isB ? 768 : 0);
    const unsigned short* bsrc = wqkv + (size_t)(64 * nb + 16 * w) * 1152 + 384;
    const unsigned short* osrc = wo + (size_t)(96 * w + (l >> 3)) * 384 + 64 * nb + 8 * (l & 7);
    const unsigned la0 = (unsigned)(size_t)la + w * 12288, lb0 = (unsigned)(size_t)lb + w * 12288;
#pragma unroll
    for (int j = 0; j < 3; ++j) {
      const int id = 64 * j + l, row = id / 48, pos = id - 48 * row;
#pragma unroll
      for (int g = 0; g < 4; ++g) {
        const int rw = 4 * g + row;
        const int off = rw * 1152 + 8 * ((pos & ~15) | ((pos ^ rw) & 15));
        dma1p(asrc + off, la0 + (3 * g + j) * 1024);
        if (!isB) dma1p(bsrc + off, lb0 + (3 * g + j) * 1024);
        else dma1p(osrc + (size_t)(8 * (3 * g + j)) * 384, lb0 + (3 * g + j) * 1024);
      }
    }
  }
  STAMP(1);
  const int ct = w & 1, nt = w >> 1;
  const int ra = 32 * ct + perm23(r), rb = 32 * nt + perm23(r);
  int aa[8], ab[8];
#pragma unroll
  for (int k = 0; k < 8; ++k) { aa[k] = ra * 768 + (((2 * k + h) ^ ra) & 15) * 16; ab[k] = rb * 768 + (((2 * k + h) ^ rb) & 15) * 16; }
  const int blk = (l >> 4) & 1, q = (l & 15) >> 2, p = l & 3;
  const char* tb = lb + (8 * h + q) * 128 + (32 * nt + 16 * blk + 8 * (p & 1) + 4 * (p >> 1)) * 2;
  asm volatile("s_waitcnt vmcnt(0)\n\ts_barrier" ::: "memory");
  STAMP(3);
  f32x16 acc = {};
  bf16x8 af[24], bf[24];
  auto rd = [&](auto I, auto ISB) {
    constexpr int ks = I.v;
    af[ks] = *(const bf16x8*)(la + aa[ks & 7] + (ks >> 3) * 256);
    if constexpr (ISB.v) bf[ks] = tr_frag(tb + ks * 2048, tb + ks * 2048 + 512);
    else bf[ks] = *(const bf16x8*)(lb + ab[ks & 7] + (ks >> 3) * 256);
  };
  auto gemm = [&](auto ISB) {
    sfor<0, 4>([&](auto I) { rd(I, ISB); });
    SB();
    sfor<0, 24>([&](auto I) {
      if constexpr (I.v + 4 < 24) rd(IC<I.v + 4>{}, ISB);
      acc = MFMA(af[I.v], bf[I.v], acc);
      SB();
    });
  };
  if (!isB) {
    gemm(IC<0>{});
    const float sc = 0.05103103630798288f * 1.4426950408889634f;
#pragma unroll
    for (int i = 0; i < 16; ++i) acc[i] *= sc;
  } else gemm(IC<1>{});
  STAMP(4);
  char* dst = ws + (size_t)prod * WBYTES;
  const int ntile = 2 * nb + nt, ctile = 2 * cb + ct;
#pragma unroll
  for (int s = 0; s < 2; ++s) {
    const int ks = 2 * ctile + s;
    const int idx = (prod < 2) ? ntile * 24 + ks : ((ntile / 3) * 24 + ks) * 3 + ntile % 3;
    gstore(dst + (size_t)idx * 1024 + l * 16, __builtin_bit_cast(u32x4, pack8(acc, s)));
  }
  STAMP(5);
  STOUT(ws + OFF_X1 + (size_t)TF * NTILE * TILEB + 2097152);
}

DI void partial_stats(const f32x2 (&v)[8], char* st, int r, int w, int h) {
  f32x2 s = v[0];
#pragma unroll
  for (int i = 1; i < 8; ++i) s += v[i];
  const float mean_w = xsum(s[0] + s[1]) * (1.f / 32.f);
  f32x2 q = {0.f, 0.f};
#pragma unroll
  for (int i = 0; i < 8; ++i) { const f32x2 d = v[i] - mean_w; q += d * d; }
  const float m2_w = xsum(q[0] + q[1]);
  *(float*)(st + r * 112 + h * 48 + w * 4) = h ? m2_w : mean_w;
}
DI void combine_stats(const char* st, int r, float& mean, float& rstd, float& vare) {
  f32x4 a[6];
#pragma unroll
  for (int i = 0; i < 6; ++i) a[i] = *(const f32x4*)(st + r * 112 + 16 * i);
  const f32x4 ms = a[0] + a[1] + a[2], qs = a[3] + a[4] + a[5];
  const float m = (ms[0] + ms[1] + ms[2] + ms[3]) * (1.f / 12.f);
  const f32x4 d0 = a[0] - m, d1 = a[1] - m, d2 = a[2] - m;
  const f32x4 e = qs + 32.f * (d0 * d0 + d1 * d1 + d2 * d2);
  mean = m;
  vare = (e[0] + e[1] + e[2] + e[3]) * (1.f / 384.f) + 1e-5f;
  rstd = __builtin_amdgcn_rsqf(vare);
}
template <int N> DI void wait2(u32x4 (&x)[2]) { asm volatile("s_waitcnt vmcnt(%2)" : "+v"(x[0]), "+v"(x[1]) : "i"(N)); }

__global__ __launch_bounds__(768, 3) void k1_temporal(const unsigned short* __restrict__ x, const char* __restrict__ WA,
                                                      const char* __restrict__ WB, char* __restrict__ H2, char* __restrict__ X1) {
  __shared__ __attribute__((aligned(16))) char lds[2 * TILEB + 2048 + 2 * 3584];
  char* himg = lds;
  char* qimg = lds + TILEB;
  char* amix = lds + 2 * TILEB;
  char* st1 = lds + 2 * TILEB + 2048;
  char* st2 = st1 + 3584;
  const int tid = threadIdx.x, w = __builtin_amdgcn_readfirstlane(tid >> 6), l = tid & 63, r = l & 31, h = l >> 5;
  const int pos = r & 3, t = r >> 2, n = 4 * blockIdx.x + pos;
  const bool valid = (r < 28) && (n < NP);
  STDECL; STAMP(0);
  asm volatile("" :: "s"(H2), "s"(X1), "s"(WB));
  bf16x8 wf[48];
  const unsigned voff = l * 16;
  const char* wa4 = WA + (size_t)w * 24 * 1024 + 4096;
  const char* wb4 = WB + (size_t)w * 24 * 1024 + 4096;
  auto wissue = [&](auto I) {
    constexpr int f = I.v;
    if constexpr (f < 24) wload<f, false>(wf[f], voff, wa4);
    else if constexpr (f < 48) wload<f - 24, false>(wf[f], voff, wb4);
  };
  const int so0 = slot_off(0, h, r), so1 = slot_off(1, h, r);
  u32x4 xr[2];
  {
#pragma unroll
    for (int i = 0; i < 2; ++i) {
      const int id = (2 * w + i) * 64 + l, row0 = id / 48, ps = id - 48 * row0;
      const int ch = (ps & ~15) | ((ps ^ row0) & 15);
      const int row = (row0 < 28) ? row0 : 27, nn = min(4 * (int)blockIdx.x + (row & 3), NP - 1);
      dma1p(x + ((size_t)(row >> 2) * NP + nn) * CH + 8 * ch, (unsigned)(size_t)qimg + (2 * w + i) * 1024);
    }
    sfor<0, 6>(wissue);
    wait_vm<6>();
    asm volatile("s_barrier" ::: "memory");
    STAMP(1);
    const char* xp = qimg + r * 768 + (w >> 2) * 256;
    xr[0] = *(const u32x4*)(xp + (((4 * w + h) ^ r) & 15) * 16); xr[1] = *(const u32x4*)(xp + (((4 * w + 2 + h) ^ r) & 15) * 16);
#pragma unroll
    for (int i = 0; i < 2; ++i) if (!valid) xr[i] = u32x4{0, 0, 0, 0};
    f32x2 v[8];
#pragma unroll
    for (int i = 0; i < 8; ++i) v[i] = unpk(xr[i >> 2][i & 3]);
    partial_stats(v, st1, r, w, h);
    SB();
    sfor<6, 10>(wissue);
    lds_barrier();
    float mean, rstd, vare;
    combine_stats(st1, r, mean, rstd, vare);
    const float nb = -mean * rstd;
#pragma unroll
    for (int i = 0; i < 2; ++i) {
      u32x4 p;
#pragma unroll
      for (int j = 0; j < 4; ++j) p[j] = pk(v[4 * i + j] * rstd + nb);
      *(u32x4*)(himg + (2 * w + i) * 1024 + (i ? so1 : so0)) = p;
    }
    SB();
    sfor<10, 14>(wissue);
    lds_barrier();
  }
  STAMP(2);
  f32x16 acc = {};
  {
    bf16x8 hf[24];
    hf[0] = *(const bf16x8*)(himg + so0); hf[1] = *(const bf16x8*)(himg + 1024 + so1);
    SB();
    sfor<0, 24>([&](auto I) {
      constexpr int ks = I.v;
      if constexpr (ks + 2 < 24) hf[ks + 2] = *(const bf16x8*)(himg + (ks + 2) * 1024 + ((ks & 1) ? so1 : so0));
      constexpr int iss = (ks < 10) ? 15 + ks : 24 + (ks - 9) / 2;
      if constexpr (ks < 10 || (ks & 1)) wissue(IC<iss - 1>{});
      wwait<iss - 1 - ks, false>(wf[ks]);
      acc = MFMA(wf[ks], hf[ks], acc);
      SB();
    });
  }
  STAMP(3);
  *(bf16x8*)(qimg + (2 * w) * 1024 + l * 16) = pack8(acc, 0);
  *(bf16x8*)(qimg + (2 * w + 1) * 1024 + l * 16) = pack8(acc, 1);
  lds_barrier();
  if (w == 0) {
    f32x16 sc;
#pragma unroll
    for (int i = 0; i < 16; ++i) {
      const int row = (i & 3) + 8 * (i >> 2) + 4 * h;
      sc[i] = ((i & 3) == pos && row < 28) ? 0.f : -3.0e38f;
    }
    bf16x8 ha[24], qb[24];
    sfor<0, 5>([&](auto I) {
      ha[I.v] = *(const bf16x8*)(himg + I.v * 1024 + ((I.v & 1) ? so1 : so0));
      qb[I.v] = *(const bf16x8*)(qimg + I.v * 1024 + l * 16);
    });
    SB();
    sfor<0, 24>([&](auto I) {
      constexpr int ks = I.v;
      if constexpr (ks + 5 < 24) {
        ha[ks + 5] = *(const bf16x8*)(himg + (ks + 5) * 1024 + (((ks + 5) & 1) ? so1 : so0));
        qb[ks + 5] = *(const bf16x8*)(qimg + (ks + 5) * 1024 + l * 16);
      }
      sc = MFMA(ha[ks], qb[ks], sc);
      SB();
    });
    float mx = sc[0];
#pragma unroll
    for (int i = 1; i < 16; ++i) mx = fmaxf(mx, sc[i]);
    mx = xmax(mx);
    float sum = 0.f;
#pragma unroll
    for (int i = 0; i < 16; ++i) { sc[i] = __builtin_amdgcn_exp2f(sc[i] - mx); sum += sc[i]; }
    const float inv = __builtin_amdgcn_rcpf(xsum(sum));
#pragma unroll
    for (int i = 0; i < 16; ++i) sc[i] *= inv;
    *(bf16x8*)(amix + l * 16) = pack8(sc, 0);
    *(bf16x8*)(amix + 1024 + l * 16) = pack8(sc, 1);
    lds_barrier();
  }
  SB();
  sfor<31, 44>(wissue);
  if (w != 0) lds_barrier();
  STAMP(4);
  {
    const int blk = (l >> 4) & 1, q = (l & 15) >> 2, p = l & 3, hh = p & 1;
    const int xr4 = 4 * (2 * blk + hh);
    const char* tb0 = himg + w * 2048 + blk * 1024 + (hh * 32 + ((4 * h + q) ^ xr4)) * 16 + 8 * (p >> 1);
    const char* tb1 = himg + w * 2048 + blk * 1024 + (hh * 32 + ((8 + 4 * h + q) ^ xr4)) * 16 + 8 * (p >> 1);
    f32x16 a = {};
    a = MFMA(tr_frag(tb0, tb1), *(const bf16x8*)(amix + l * 16), a);
    a = MFMA(tr_frag(tb0 + 256, tb1 + 256), *(const bf16x8*)(amix + 1024 + l * 16), a);
    *(bf16x8*)(qimg + (2 * w) * 1024 + l * 16) = pack8(a, 0);
    *(bf16x8*)(qimg + (2 * w + 1) * 1024 + l * 16) = pack8(a, 1);
  }
  lds_barrier();
  STAMP(5);
  acc = f32x16{};
  {
    bf16x8 hf[24];
    hf[0] = *(const bf16x8*)(qimg + l * 16); hf[1] = *(const bf16x8*)(qimg + 1024 + l * 16);
    SB();
    sfor<0, 24>([&](auto I) {
      constexpr int ks = I.v;
      if constexpr (ks + 2 < 24) hf[ks + 2] = *(const bf16x8*)(qimg + (ks + 2) * 1024 + l * 16);
      if constexpr (ks < 4) wissue(IC<44 + ks>{});
      wwait<((ks < 4) ? 45 + ks : 48) - 1 - (24 + ks), false>(wf[24 + ks]);
      acc = MFMA(wf[24 + ks], hf[ks], acc);
      SB();
    });
  }
  STAMP(6);
  {
    f32x2 v[8];
#pragma unroll
    for (int i = 0; i < 8; ++i) v[i] = unpk(pk(unpk(xr[i >> 2][i & 3]) + f32x2{acc[2 * i], acc[2 * i + 1]}));
    partial_stats(v, st2, r, w, h);
    const int rr = n & 31;
    const size_t tile = ((size_t)t * NTILE + (n >> 5)) * TILEB + (size_t)(2 * w) * 1024;
    const int d0 = slot_off(0, h, rr), d1 = slot_off(1, h, rr);
    lds_barrier();
    float mean, rstd, vare;
    combine_stats(st2, r, mean, rstd, vare);
    const float nb = -mean * rstd;
    if (r < 28) {
#pragma unroll
      for (int i = 0; i < 2; ++i) {
        u32x4 p;
#pragma unroll
        for (int j = 0; j < 4; ++j) p[j] = pk(v[4 * i + j] * rstd + nb);
        gstore(H2 + tile + i * 1024 + (i ? d1 : d0), p);
      }
      if (w == 0 && h == 0) *(f32x2*)(X1 + ((size_t)t * (32 * NTILE) + n) * 8) = f32x2{mean, vare * rstd};
    }
  }
  STAMP(7);
  STOUT(X1 + (size_t)TF * NTILE * TILEB);
}

constexpr bool w2a(int f) { return f < 16 || f >= 52; }
DI void mfma_s(f32x16& acc, const bf16x8& a, const bf16x8& b) {
  asm volatile("v_mfma_f32_32x32x16_bf16 %0, %1, %2, %0" : "+v"(acc) : "v"(a), "v"(b));
}
DI void mfma_s_end(f32x16& acc, const bf16x8& a, const bf16x8& b) {
  asm volatile("v_mfma_f32_32x32x16_bf16 %0, %1, %2, %0\n\ts_nop 7\n\ts_nop 7" : "+v"(acc) : "v"(a), "v"(b));
}
DI void mfma_s0(f32x16& acc, const bf16x8& a, const bf16x8& b) {
  asm volatile("s_nop 1\n\tv_mfma_f32_32x32x16_bf16 %0, %1, %2, 0" : "=&v"(acc) : "v"(a), "v"(b));
}
DI float max3(float a, float b, float c) { float r; asm("v_max3_f32 %0, %1, %2, %3" : "=v"(r) : "v"(a), "v"(b), "v"(c)); return r; }
constexpr int ev_slot(int e) { return e < 4 ? 2 * e + 1 : e < 20 ? 16 + 3 * ((e - 4) / 2) + ((e - 4) & 1) : 41 + 2 * (e - 20); }
constexpr int ev_at(int slot) { for (int e = 0; e < 24; ++e) if (ev_slot(e) == slot) return e; return -1; }
constexpr int ev_before(int slot) { int n = 0; for (int e = 0; e < 24; ++e) n += ev_slot(e) < slot; return n; }

__global__ __launch_bounds__(256, 1) void k2_spatial(const char* __restrict__ WA, const char* __restrict__ WB,
                                                     const char* __restrict__ H2, const char* __restrict__ X1,
                                                     unsigned short* __restrict__ out) {
  __shared__ __attribute__((aligned(16))) char lds[163840];
  const int t = blockIdx.x & 7, qt = blockIdx.x >> 3;
  if (t == 7) return;
  const int tid = threadIdx.x, w = __builtin_amdgcn_readfirstlane(tid >> 6), l = tid & 63, r = l & 31, h = l >> 5;
  asm volatile("" :: "s"(WB), "s"(X1), "s"(out));
  const unsigned voff = l * 16;
  const int so0 = slot_off(0, h, r), so1 = slot_off(1, h, r);
  unsigned vu[4];
  vu[0] = voff + (0 ^ w) * 2048 + 2048; vu[1] = voff + (1 ^ w) * 2048;
  vu[2] = voff + (2 ^ w) * 2048 + 2048; vu[3] = voff + (3 ^ w) * 2048;
  const char* keysm = H2 + (size_t)t * NTILE * TILEB - 2048;
  const int nt = (w < 3) ? 6 : 5;
  char* ring = lds + w * 40960;
  const unsigned ring0 = (unsigned)(size_t)ring;
  const char* wa4 = WA + (size_t)w * 72 * 1024 + 4096;
  const char* wb4 = WB + (size_t)w * 72 * 1024 + 4096;
  auto tile_of = [&](int jj) { const int i = 4 * jj + w + qt; return (i >= NTILE) ? i - NTILE : i; };
  auto dma_k = [&](auto K, const char* segsrc, unsigned m) {
    constexpr int k = K.v, cc = k >> 1;
    dma1<(k & 3) * 1024>(segsrc, m + (cc >= 2 ? 4096 : 0), vu[cc]);
  };
  u32x4 cb = {h ? 0u : 0xff610000u, 0u, 0u, 0u};
  auto sstart = [&](f32x16& sa, int tile) {
    const bool msk = (tile == NTILE - 1) && (r >= NP - 32 * (NTILE - 1));
    const u32x4 ca = {h ? 0u : (msk ? 0x3f803f80u : 0x3f80u), 0u, 0u, 0u};
    mfma_s0(sa, __builtin_bit_cast(bf16x8, ca), __builtin_bit_cast(bf16x8, cb));
  };

  STDECL; STAMP(0);
  bf16x8 qf[24];
  f32x16 scur, snext;
  bf16x8 kc[2];
  {
    constexpr int WIN = 30;
    bf16x8 wf[72];
    {
      const char* src = keysm + 2048 + (size_t)qt * TILEB + w * 6144;
      sfor<0, 4>([&](auto I) { dma1<I.v * 1024>(src, ring0 + 4 * 8192, voff); });
      sfor<0, 2>([&](auto I) { dma1<I.v * 1024>(src + 4096, ring0 + 4 * 8192 + 4096, voff); });
    }
    const char* src0 = keysm + (size_t)tile_of(0) * TILEB;
    const char* src1 = keysm + (size_t)tile_of(1) * TILEB;
    auto stream = [&](auto I) {
      if constexpr (I.v < 72) wload<I.v, true>(wf[I.v], voff, wa4);
      else if constexpr (I.v < 96) dma_k(IC<(I.v - 72) & 7>{}, src0 + ((I.v - 72) >> 3) * 8192, ring0 + ((I.v - 72) >> 3) * 8192);
      else dma_k(IC<(I.v - 96) & 7>{}, src1 + ((I.v - 96) >> 3) * 8192, ring0 + (3 + ((I.v - 96) >> 3)) * 8192);
    };
    sfor<0, 12>(stream);
    STAMP(1);
    wait_vm<12>();
    asm volatile("s_barrier" ::: "memory");
    sfor<12, WIN>(stream);
    STAMP(2);
    f32x16 acc[3] = {};
    bf16x8 hf[24];
    auto hread = [&](auto I) {
      hf[I.v] = *(const bf16x8*)(lds + (I.v / 6) * 40960 + 4 * 8192 + (I.v % 6) * 1024 + ((I.v & 1) ? so1 : so0));
    };
    sfor<0, 2>(hread);
    SB();
    sfor<0, 24>([&](auto I) {
      constexpr int ks = I.v;
      if constexpr (ks + 2 < 24) hread(IC<ks + 2>{});
      constexpr int i0 = (ks < 14) ? 3 * ks + WIN : 72 + 2 * (ks - 14), i1 = (ks < 14) ? i0 + 3 : i0 + 2;
      sfor<i0, i1>(stream);
      wwait<i1 - 3 * ks - 3, true>(wf[3 * ks + 2]); wtie<true>(wf[3 * ks]); wtie<true>(wf[3 * ks + 1]);
#pragma unroll
      for (int i = 0; i < 3; ++i) acc[i] = MFMA(wf[3 * ks + i], hf[ks], acc[i]);
      SB();
    });
    STAMP(3);
    sfor<0, 3>([&](auto I) {
#pragma unroll
      for (int s = 0; s < 2; ++s) *(bf16x8*)(ring + 3 * 8192 + (2 * I.v + s) * 1024 + l * 16) = pack8(acc[I.v], s);
      SB();
      stream(IC<92 + I.v>{});
    });
    lds_barrier();
    sfor<0, 4>([&](auto I) {
#pragma unroll
      for (int u = 3 * I.v; u < 3 * I.v + 3; ++u) {
        const int dt = (u & ~3) + ((u & 3) ^ w), ow = dt / 3;
        const char* p = lds + ow * 40960 + 3 * 8192 + (dt - 3 * ow) * 2048 + l * 16;
        qf[2 * u] = *(const bf16x8*)p; qf[2 * u + 1] = *(const bf16x8*)(p + 1024);
      }
      SB();
      if constexpr (I.v == 0) stream(IC<95>{});
    });
    lds_barrier();
    STAMP(4);
    bf16x8 kf[26];
    auto kread = [&](auto I) {
      constexpr int f = I.v;
      if constexpr ((f & 1) == 0) wait_vm<23 + ((f > 2) ? (f - 2) / 2 : 0) - (f + 1)>();
      kf[f] = *(const bf16x8*)(ring + f * 1024 + ((f & 1) ? so1 : so0));
    };
    sfor<0, 2>(kread);
    sstart(scur, tile_of(0));
    SB();
    sfor<0, 24>([&](auto I) {
      constexpr int f = I.v;
      kread(IC<f + 2>{});
      if constexpr (f & 1) stream(IC<96 + f / 2>{});
      if constexpr (f < 23) mfma_s(scur, kf[f], qf[f]); else mfma_s_end(scur, kf[f], qf[f]);
      SB();
    });
    kc[0] = kf[24]; kc[1] = kf[25];
  }

  const int blk = (l >> 4) & 1, q4 = (l & 15) >> 2, p4 = l & 3, hh = p4 & 1;
  const int xr4 = 4 * (2 * blk + hh);
  const int to0 = blk * 1024 + (hh * 32 + ((4 * h + q4) ^ xr4)) * 16 + 8 * (p4 >> 1);
  const int to1 = blk * 1024 + (hh * 32 + ((8 + 4 * h + q4) ^ xr4)) * 16 + 8 * (p4 >> 1);

  f32x16 o[12];
#pragma unroll
  for (int c = 0; c < 12; ++c) o[c] = f32x16{};
  float mrun = 0.f;
  f32x2 lsum = {0.f, 0.f};
  bf16x8 w2[72];
  auto w2issue = [&](auto I) { if constexpr (I.v < 72) wload<I.v, w2a(I.v)>(w2[I.v], voff, wb4); };

  auto period = [&](auto MODEC, int jj, int slot) {
    constexpr int MODE = MODEC.v;
    constexpr int PFD = 4;
    const int s1 = (slot + 1 >= 5) ? slot - 4 : slot + 1, s2 = (slot + 2 >= 5) ? slot - 3 : slot + 2;
    const int s3 = (slot + 3 >= 5) ? slot - 2 : slot + 3, s4 = (slot + 4 >= 5) ? slot - 1 : slot + 4;
    char* rb[3] = {ring + slot * 8192, ring + s1 * 8192, ring + s2 * 8192};
    char* nb[4] = {ring + s3 * 8192, ring + s4 * 8192, ring + slot * 8192, ring + s1 * 8192};
    const char* srcA = keysm + (size_t)tile_of(jj + 1) * TILEB;
    const char* srcB = keysm + (size_t)tile_of(jj + 2) * TILEB;
    auto stream = [&](auto S) {
      constexpr int e = ev_at(S.v);
      if constexpr (MODE == 2) {
        constexpr int sl = S.v, i0 = (sl < 8) ? sl : (sl < 24) ? 8 + (sl - 8) + (sl - 7) / 2 : 8 + sl;
        if constexpr (sl < 32) w2issue(IC<12 + i0>{});
        if constexpr (sl >= 8 && sl < 24 && (sl & 1) == 0) w2issue(IC<13 + i0>{});
      } else if constexpr (e >= 0) {
        if constexpr (e < 4) dma_k(IC<4 + e>{}, srcA + 8192, ring0 + s4 * 8192);
        else if constexpr (e < 12) dma_k(IC<e - 4>{}, srcA + 16384, ring0 + slot * 8192);
        else if constexpr (MODE == 1) w2issue(IC<e - 12>{});
        else if constexpr (e < 20) dma_k(IC<e - 12>{}, srcB, ring0 + s1 * 8192);
        else dma_k(IC<e - 20>{}, srcB + 8192, ring0 + s2 * 8192);
      }
    };
    bf16x8 kf[26], tf[24];
    kf[0] = kc[0]; kf[1] = kc[1];
    auto kread = [&](auto I, auto S) {
      constexpr int f = I.v;
      if constexpr (MODE == 0 || (MODE == 1 && f < 24)) {
        if constexpr ((f & 1) == 0) wait_vm<ev_before(S.v) - 1 - (f + 1 - 12)>();
        kf[f] = *(const bf16x8*)(nb[f >> 3] + (f & 7) * 1024 + ((f & 1) ? so1 : so0));
      }
    };
    auto tread = [&](auto I) {
      constexpr int u = I.v >> 1;
      const char* b = rb[u >> 2] + (u & 3) * 2048 + (I.v & 1) * 256;
      tf[I.v] = tr_frag(b + to0, b + to1);
    };
    float mx;
    if constexpr (MODE != 2) sstart(snext, tile_of(jj + 1));
    sfor<0, 2>([&](auto I) {
      kread(IC<I.v + 2>{}, I);
      if constexpr (MODE != 2) mfma_s(snext, kf[I.v], qf[I.v]);
      if constexpr (I.v == 0) mx = max3(max3(max3(max3(scur[0], scur[1], scur[2]), scur[3], scur[4]), scur[5], scur[6]), scur[7], scur[8]);
      if constexpr (I.v == 1) {
        mx = max3(max3(max3(max3(mx, scur[9], scur[10]), scur[11], scur[12]), scur[13], scur[14]), scur[15], scur[15]);
        const unsigned u = __builtin_bit_cast(unsigned, mx);
        const auto sw = __builtin_amdgcn_permlane32_swap(u, u, false, false);
        mx = max3(__builtin_bit_cast(float, (unsigned)sw[0]), __builtin_bit_cast(float, (unsigned)sw[1]), mx);
      }
      stream(I);
      SB();
    });
    if (jj == 0 || __any(mx > 64.f)) {
      const unsigned mb = pack2(mrun + ((jj == 0) ? mx : fmaxf(mx, 0.f)), 0.f) & 0xffffu;
      const float dl = bflo(mb) - mrun;
      mrun = bflo(mb);
      cb[0] = h ? 0u : (0xff610000u | (mb ^ 0x8000u));
#pragma unroll
      for (int i = 0; i < 16; ++i) scur[i] -= dl;
      if constexpr (MODE != 2) {
        asm volatile("s_nop 7\n\ts_nop 7\n\tv_sub_f32 %0, %0, %1" : "+v"(snext[0]) : "v"(dl));
#pragma unroll
        for (int i = 1; i < 15; ++i) asm volatile("v_sub_f32 %0, %0, %1" : "+v"(snext[i]) : "v"(dl));
        asm volatile("v_sub_f32 %0, %0, %1\n\ts_nop 1" : "+v"(snext[15]) : "v"(dl));
      }
      if (jj != 0) {
        const float f = __builtin_amdgcn_exp2f(-dl);
        lsum *= f;
#pragma unroll
        for (int c = 0; c < 12; ++c)
#pragma unroll
          for (int i = 0; i < 16; ++i) {
            float tmp;
            asm volatile("v_accvgpr_read_b32 %1, %0\n\tv_mul_f32 %1, %1, %2\n\tv_accvgpr_write_b32 %0, %1" : "+a"(o[c][i]), "=&v"(tmp) : "v"(f));
          }
        asm volatile("s_nop 4");
      }
    }
    SB();
    bf16x8 pf0, pf1;
    sfor<2, 8>([&](auto I) {
      constexpr int f = I.v;
      if constexpr (f + 2 < 8) kread(IC<f + 2>{}, I);
      if constexpr (f >= 4) tread(IC<f - 4>{});
      if constexpr (MODE != 2) mfma_s(snext, kf[f], qf[f]);
      if constexpr (f < 6) {
#pragma unroll
        for (int i = 3 * (f - 2); i < 3 * (f - 2) + 3; ++i) scur[i] = __builtin_amdgcn_exp2f(scur[i]);
      } else {
        scur[2 * f] = __builtin_amdgcn_exp2f(scur[2 * f]); scur[2 * f + 1] = __builtin_amdgcn_exp2f(scur[2 * f + 1]);
        if constexpr (f == 6) pf0 = pack8(scur, 0);
      }
      stream(I);
      SB();
    });
    auto oput = [&](auto I) {
      constexpr int u = I.v, k = 3 * (u >> 2) + (u & 3) - 1;
      if constexpr ((u & 3) != 0) {
        char* dst = ((k < 4) ? nb[k >> 1] : rb[(k - 4) >> 1]) + (k & 1) * 4096 + l * 16;
#pragma unroll
        for (int g = 0; g < 4; ++g) *(f32x4*)(dst + g * 1024) = f32x4{o[u][4 * g], o[u][4 * g + 1], o[u][4 * g + 2], o[u][4 * g + 3]};
      }
    };
    sfor<0, 24>([&](auto I) {
      constexpr int p = I.v;
      if constexpr (p + PFD < 24) tread(IC<p + PFD>{});
      if constexpr (p >= 22) kread(IC<p - 14>{}, IC<8 + p>{});
      if constexpr (p == 0) pf1 = pack8(scur, 1);
      o[p >> 1] = MFMA(tf[p], (p & 1) ? pf1 : pf0, o[p >> 1]);
      if constexpr (p >= 2 && p < 10) lsum += f32x2{scur[2 * (p - 2)], scur[2 * (p - 2) + 1]};
      stream(IC<8 + p>{});
      if constexpr (MODE == 2 && p >= 5 && (p & 1)) oput(IC<(p - 3) / 2>{});
      SB();
    });
    if constexpr (MODE == 2) oput(IC<11>{});
    else {
      sfor<8, 24>([&](auto I) {
        constexpr int f = I.v;
        kread(IC<f + 2>{}, IC<24 + f>{});
        if constexpr (f < 23) { mfma_s(snext, kf[f], qf[f]); stream(IC<24 + f>{}); }
        else { stream(IC<24 + f>{}); mfma_s_end(snext, kf[f], qf[f]); }
        SB();
      });
      if constexpr (MODE == 0) { kc[0] = kf[24]; kc[1] = kf[25]; }
      scur = snext;
    }
  };
  int slot = 0;
  for (int jj = 0; jj < nt - 2; ++jj) {
    period(IC<0>{}, jj, slot);
    slot = (slot + 3 >= 5) ? slot - 2 : slot + 3;
  }
  STAMP(5);
  period(IC<1>{}, nt - 2, slot);
  slot = (slot + 3 >= 5) ? slot - 2 : slot + 3;
  STAMP(6);
  period(IC<2>{}, nt - 1, slot);
  STAMP(7);

  const float ltot = xsum(lsum[0] + lsum[1]);
  auto oslot = [&](int sw, int k) {
    const int b = (sw < 3) ? 0 : 4;
    const int i = b + k + 6;
    return lds + sw * 40960 + ((i >= 10) ? i - 10 : i) * 4096;
  };
  if (h == 0) *(f32x2*)(oslot(w, 9) + r * 8) = f32x2{mrun, ltot};
  SB();
  sfor<52, 54>(w2issue);
  lds_barrier();
  float fs[4];
  {
    const f32x2 a = *(const f32x2*)(oslot(w, 9) + r * 8), b = *(const f32x2*)(oslot(w ^ 1, 9) + r * 8);
    const f32x2 c = *(const f32x2*)(oslot(w ^ 2, 9) + r * 8), d = *(const f32x2*)(oslot(w ^ 3, 9) + r * 8);
    const float m = fmaxf(fmaxf(a[0], b[0]), fmaxf(c[0], d[0]));
    fs[0] = __builtin_amdgcn_exp2f(a[0] - m); fs[1] = __builtin_amdgcn_exp2f(b[0] - m);
    fs[2] = __builtin_amdgcn_exp2f(c[0] - m); fs[3] = __builtin_amdgcn_exp2f(d[0] - m);
    const float inv = __builtin_amdgcn_rcpf(a[1] * fs[0] + b[1] * fs[1] + c[1] * fs[2] + d[1] * fs[3]);
    fs[0] *= inv; fs[1] *= inv; fs[2] *= inv; fs[3] *= inv;
  }
  f32x4 pa[4][3];
  u32x4 opk[2];
  auto mread = [&](auto SG, auto G) {
#pragma unroll
    for (int cc = 1; cc < 4; ++cc) pa[G.v][cc - 1] = *(const f32x4*)(oslot(w ^ cc, 3 * SG.v + cc - 1) + l * 16 + G.v * 1024);
  };
  auto mstep = [&](auto SG, auto G) {
    constexpr int sg = SG.v, g = G.v;
    f32x4 own;
#pragma unroll
    for (int e = 0; e < 4; ++e) {
      float tmp;
      asm volatile("v_accvgpr_read_b32 %0, %1" : "=v"(tmp) : "a"(o[4 * sg][4 * g + e]));
      own[e] = tmp;
    }
    own = own * fs[0] + pa[g][0] * fs[1] + pa[g][1] * fs[2] + pa[g][2] * fs[3];
    opk[g >> 1][2 * (g & 1)] = pack2(own[0], own[1]); opk[g >> 1][2 * (g & 1) + 1] = pack2(own[2], own[3]);
    if constexpr (g == 3) {
      char* dst = oslot(w ^ 1, 3 * sg) + l * 16;
      *(u32x4*)dst = opk[0]; *(u32x4*)(dst + 1024) = opk[1];
    }
  };
  sfor<0, 4>([&](auto G) { mread(IC<0>{}, G); });
  SB();
  sfor<54, 56>(w2issue);
  sfor<0, 4>([&](auto G) { mstep(IC<0>{}, G); });
  lds_barrier();
  STAMP(8);
  f32x16 acc[3] = {};
  u32x4 xv[6];
  f32x2 ms;
  {
    const char* x1 = H2 + ((size_t)t * NTILE + qt) * TILEB + (size_t)(6 * w) * 1024 + 2048;
    const char* xa = x1 + so0;
    const char* xb = x1 + so1;
    bf16x8 of[24];
    auto oread = [&](auto I) {
      constexpr int ks = I.v;
      of[ks] = *(const bf16x8*)(oslot(((ks >> 1) & 3) ^ 1, 3 * (ks >> 3)) + (ks & 1) * 1024 + l * 16);
    };
    sfor<0, 3>([&](auto SG) {
      constexpr int sg = SG.v;
      sfor<8 * sg, 8 * sg + 2>(oread);
      SB();
      sfor<0, 8>([&](auto K) {
        constexpr int k = K.v, ks = 8 * sg + k;
        if constexpr (k + 2 < 8) oread(IC<ks + 2>{});
        if constexpr (sg < 2 && k < 4) mread(IC<sg + 1>{}, K);
        if constexpr (ks < 8) sfor<56 + 2 * ks, 58 + 2 * ks>(w2issue);
        else if constexpr (ks < 14) gload_p<(ks - 8) * 1024 - 2048>(xv[ks - 8], ((ks - 8) & 1) ? xb : xa);
        else if constexpr (ks == 14)
          asm volatile("global_load_dwordx2 %0, %1, off" : "=v"(ms) : "v"(X1 + ((size_t)t * (32 * NTILE) + 32 * qt + r) * 8));
        constexpr int issued = (ks < 8) ? 58 + 2 * ks : ((ks < 15) ? 72 + (ks - 7) : 79);
        wwait<issued - 3 * ks - 3, w2a(3 * ks + 2)>(w2[3 * ks + 2]); wtie<w2a(3 * ks)>(w2[3 * ks]); wtie<w2a(3 * ks + 1)>(w2[3 * ks + 1]);
#pragma unroll
        for (int i = 0; i < 3; ++i) acc[i] = MFMA(w2[3 * ks + i], of[ks], acc[i]);
        if constexpr (sg < 2 && k >= 2 && k < 6) mstep(IC<sg + 1>{}, IC<k - 2>{});
        SB();
      });
      if constexpr (sg < 2) lds_barrier();
    });
  }
  STAMP(9);
  wait6<0>(xv);
  asm volatile("" : "+v"(ms));
  {
    char* ob0 = oslot(w ^ 2, 1);
    char* ob1 = oslot(w ^ 3, 2) - 16 * 208;
#pragma unroll
    for (int i = 0; i < 6; ++i) {
      u32x4 p;
#pragma unroll
      for (int j = 0; j < 4; ++j)
        p[j] = pk(unpk(xv[i][j]) * ms[1] + (ms[0] + f32x2{acc[i >> 1][8 * (i & 1) + 2 * j], acc[i >> 1][8 * (i & 1) + 2 * j + 1]}));
      *(u32x4*)(((r < 16) ? ob0 : ob1) + r * 208 + i * 32 + h * 16) = p;
    }
    unsigned short* obase = out + ((size_t)t * NP + 32 * qt) * CH + 96 * w;
#pragma unroll
    for (int i = 0; i < 6; ++i) {
      const int id = i * 64 + l, row = id / 12, ch = id - 12 * row;
      const u32x4 p = *(const u32x4*)(((i < 3) ? ob0 : ob1) + row * 208 + ch * 16);
      if (32 * qt + row < NP) *(u32x4*)(obase + (size_t)row * CH + 8 * ch) = p;
    }
  }
  STAMP(10);
  STOUT((char*)X1 + (size_t)TF * NTILE * TILEB + 1048576);
}

extern "C" void kernel_launch(void* const* d_in, const int* in_sizes, int n_in,
                              void* d_out, int out_size, void* d_ws, size_t ws_size,
                              hipStream_t stream) {
  const unsigned short* x = (const unsigned short*)d_in[0];
  const unsigned short* wqkv_t = (const unsigned short*)d_in[1];
  const unsigned short* wo_t = (const unsigned short*)d_in[2];
  const unsigned short* wqkv_s = (const unsigned short*)d_in[3];
  const unsigned short* wo_s = (const unsigned short*)d_in[4];
  char* ws = (char*)d_ws;
  k0_fold<<<144, 256, 0, stream>>>(wqkv_t, wo_t, wqkv_s, wo_s, ws);
  k1_temporal<<<184, 768, 0, stream>>>(x, ws + OFF_WA_T, ws + OFF_WB_T, ws + OFF_H2, ws + OFF_X1);
  k2_spatial<<<184, 256, 0, stream>>>(ws + OFF_WA_S, ws + OFF_WB_S, ws + OFF_H2, ws + OFF_X1, (unsigned short*)d_out);
}
```

```cpp
#include <hip/hip_runtime.h>
#include <hip/hip_bf16.h>

using bf16x8 = __attribute__((ext_vector_type(8))) __bf16;
using bf16x2 = __attribute__((ext_vector_type(2))) __bf16;
using s16x4  = __attribute__((ext_vector_type(4))) short;
using s16x8  = __attribute__((ext_vector_type(8))) short;
using f32x16 = __attribute__((ext_vector_type(16))) float;
using f32x4  = __attribute__((ext_vector_type(4))) float;
using f32x2  = __attribute__((ext_vector_type(2))) float;
using u32x4  = __attribute__((ext_vector_type(4))) unsigned;
typedef __attribute__((address_space(3))) s16x4 lds_s16x4;

#define DI __device__ __forceinline__
#define MFMA(a, b, c) __builtin_amdgcn_mfma_f32_32x32x16_bf16((a), (b), (c), 0, 0, 0)

constexpr int TF = 7, NP = 729, CH = 384;
constexpr int NTILE = 23;
constexpr int WBYTES = 12 * 24 * 1024;
constexpr int TILEB = 24 * 1024;
constexpr size_t OFF_WA_T = 0, OFF_WB_T = WBYTES, OFF_WA_S = 2 * WBYTES, OFF_WB_S = 3 * WBYTES;
constexpr size_t OFF_H2 = 4 * (size_t)WBYTES;
constexpr size_t OFF_X1 = OFF_H2 + (size_t)TF * NTILE * TILEB;

DI int perm23(int r) { return (r & 0x13) | ((r & 4) << 1) | ((r & 8) >> 1); }
DI int slot_off(int ksodd, int h, int r) { return (h * 32 + (r ^ (4 * (2 * ksodd + h)))) * 16; }

DI unsigned pack2(float a, float b) {
  f32x2 p = {a, b};
  return __builtin_bit_cast(unsigned, __builtin_convertvector(p, bf16x2));
}
DI bf16x8 pack8(const f32x16& x, int s) {
  u32x4 p;
  p[0] = pack2(x[8 * s + 0], x[8 * s + 1]); p[1] = pack2(x[8 * s + 2], x[8 * s + 3]);
  p[2] = pack2(x[8 * s + 4], x[8 * s + 5]); p[3] = pack2(x[8 * s + 6], x[8 * s + 7]);
  return __builtin_bit_cast(bf16x8, p);
}
DI float bflo(unsigned u) { return __builtin_bit_cast(float, u << 16); }
DI float bfhi(unsigned u) { return __builtin_bit_cast(float, u & 0xffff0000u); }
DI f32x2 unpk(unsigned u) { return f32x2{bflo(u), bfhi(u)}; }
DI unsigned pk(f32x2 p) { return __builtin_bit_cast(unsigned, __builtin_convertvector(p, bf16x2)); }
DI float xsum(float v) {
  const unsigned u = __builtin_bit_cast(unsigned, v);
  const auto r = __builtin_amdgcn_permlane32_swap(u, u, false, false);
  return __builtin_bit_cast(float, (unsigned)r[0]) + __builtin_bit_cast(float, (unsigned)r[1]);
}
DI float xmax(float v) {
  const unsigned u = __builtin_bit_cast(unsigned, v);
  const auto r = __builtin_amdgcn_permlane32_swap(u, u, false, false);
  return fmaxf(__builtin_bit_cast(float, (unsigned)r[0]), __builtin_bit_cast(float, (unsigned)r[1]));
}
DI void lds_barrier() { asm volatile("s_waitcnt lgkmcnt(0)\n\ts_barrier" ::: "memory"); }
#define SB() __builtin_amdgcn_sched_barrier(0)
#define STDECL
#define STAMP(i)
#define STACC(i, prev)
#define STOUT(buf)
template <int I> struct IC { static constexpr int v = I; };
template <int B, int E, class F> DI void sfor(F&& f) { if constexpr (B < E) { f(IC<B>{}); sfor<B + 1, E>(f); } }
template <int OFF> DI void gload_a(bf16x8& dst, unsigned voff, const char* sbase) {
  asm volatile("global_load_dwordx4 %0, %1, %2 offset:%3" : "=a"(dst) : "v"(voff), "s"(sbase), "i"(OFF));
}
template <int OFF> DI void gload_v(bf16x8& dst, unsigned voff, const char* sbase) {
  asm volatile("global_load_dwordx4 %0, %1, %2 offset:%3" : "=v"(dst) : "v"(voff), "s"(sbase), "i"(OFF));
}
template <int OFF> DI void gload_p(u32x4& dst, const void* p) {
  asm volatile("global_load_dwordx4 %0, %1, off offset:%2" : "=v"(dst) : "v"(p), "i"(OFF));
}
template <int IDX, bool AG> DI void wload(bf16x8& dst, unsigned voff, const char* base4k) {
  if constexpr (AG) gload_a<(IDX % 8) * 1024 - 4096>(dst, voff, base4k + (IDX / 8) * 8192);
  else gload_v<(IDX % 8) * 1024 - 4096>(dst, voff, base4k + (IDX / 8) * 8192);
}
template <int N, bool AG> DI void wwait(bf16x8& a) {
  constexpr int M = (N > 63) ? 63 : N;
  if constexpr (AG) asm volatile("s_waitcnt vmcnt(%1)" : "+a"(a) : "i"(M));
  else asm volatile("s_waitcnt vmcnt(%1)" : "+v"(a) : "i"(M));
}
template <bool AG> DI void wtie(bf16x8& a) {
  if constexpr (AG) asm volatile("" : "+a"(a)); else asm volatile("" : "+v"(a));
}
template <int N> DI void wait6(u32x4 (&x)[6]) {
  asm volatile("s_waitcnt vmcnt(%6)" : "+v"(x[0]), "+v"(x[1]), "+v"(x[2]), "+v"(x[3]), "+v"(x[4]), "+v"(x[5]) : "i"(N));
}

DI bf16x8 tr_frag(const char* p0, const char* p1) {
  s16x4 lo = __builtin_amdgcn_ds_read_tr16_b64_v4i16((lds_s16x4*)p0);
  s16x4 hi = __builtin_amdgcn_ds_read_tr16_b64_v4i16((lds_s16x4*)p1);
  s16x8 v = __builtin_shufflevector(lo, hi, 0, 1, 2, 3, 4, 5, 6, 7);
  return __builtin_bit_cast(bf16x8, v);
}

template <int IMM> DI void dma1(const char* srcm, unsigned m0v, unsigned voff) {
  asm volatile("s_mov_b32 m0, %1\n\ts_nop 0\n\tglobal_load_lds_dwordx4 %2, %0 offset:%3"
               :: "s"(srcm), "s"(m0v), "v"(voff), "i"(IMM) : "memory");
}
template <int N> DI void wait_vm() { asm volatile("s_waitcnt vmcnt(%0)" :: "i"(N) : "memory"); }

DI void gstore(void* p, u32x4 v) {
  asm volatile("global_store_dwordx4 %0, %1, off sc1\n\ts_nop 1" :: "v"(p), "v"(v) : "memory");
}
DI void dma1p(const void* p, unsigned m0v) {
  asm volatile("s_mov_b32 m0, %1\n\ts_nop 0\n\tglobal_load_lds_dwordx4 %0, off" :: "v"(p), "s"(m0v) : "memory");
}

__global__ __launch_bounds__(256) void k0_fold(const unsigned short* __restrict__ wqkv_t, const unsigned short* __restrict__ wo_t,
                                               const unsigned short* __restrict__ wqkv_s, const unsigned short* __restrict__ wo_s,
                                               char* __restrict__ ws, const unsigned short* __restrict__ x) {
  __shared__ __attribute__((aligned(16))) char lds[2 * 49152];
  char* la = lds;
  char* lb = lds + 49152;
  if (blockIdx.x >= 144) {
    const int g = blockIdx.x - 144, xc = g & 7, part = g >> 3;
    u32x4 d;
    {
      unsigned long long c0, c1;
      asm volatile("s_getpc_b64 %0\n.Lwarm_pc:\n\ts_add_u32 %1, %0, .Lcode_end-.Lwarm_pc\n" : "=s"(c0), "=s"(*(unsigned*)&c1) :: "scc");
      const unsigned len = (unsigned)c1 - (unsigned)c0;
      for (unsigned o = (part * 256 + threadIdx.x) * 64; o + 16 <= len; o += 14 * 256 * 64)
        asm volatile("global_load_dwordx4 %0, %1, off" : "=v"(d) : "v"((const char*)c0 + o));
    }
    return;
  }
  const int b = blockIdx.x, prod = b / 36, rem = b % 36, nb = rem / 6, cb = rem % 6;
  const int tid = threadIdx.x, w = __builtin_amdgcn_readfirstlane(tid >> 6), l = tid & 63, r = l & 31, h = l >> 5;
  const unsigned short* wqkv = (prod < 2) ? wqkv_t : wqkv_s;
  const unsigned short* wo = (prod < 2) ? wo_t : wo_s;
  const bool isB = prod & 1;
  STDECL; STAMP(0);
  {
    const unsigned short* asrc = wqkv + (size_t)(64 * cb + 16 * w) * 1152 + (isB ? 768 : 0);
    const unsigned short* bsrc = wqkv + (size_t)(64 * nb + 16 * w) * 1152 + 384;
    const unsigned short* osrc = wo + (size_t)(96 * w + (l >> 3)) * 384 + 64 * nb + 8 * (l & 7);
    const unsigned la0 = (unsigned)(size_t)la + w * 12288, lb0 = (unsigned)(size_t)lb + w * 12288;
#pragma unroll
    for (int j = 0; j < 3; ++j) {
      const int id = 64 * j + l, row = id / 48, pos = id - 48 * row;
#pragma unroll
      for (int g = 0; g < 4; ++g) {
        const int rw = 4 * g + row;
        const int off = rw * 1152 + 8 * ((pos & ~15) | ((pos ^ rw) & 15));
        dma1p(asrc + off, la0 + (3 * g + j) * 1024);
        if (!isB) dma1p(bsrc + off, lb0 + (3 * g + j) * 1024);
        else dma1p(osrc + (size_t)(8 * (3 * g + j)) * 384, lb0 + (3 * g + j) * 1024);
      }
    }
  }
  STAMP(1);
  const int ct = w & 1, nt = w >> 1;
  const int ra = 32 * ct + perm23(r), rb = 32 * nt + perm23(r);
  int aa[8], ab[8];
#pragma unroll
  for (int k = 0; k < 8; ++k) { aa[k] = ra * 768 + (((2 * k + h) ^ ra) & 15) * 16; ab[k] = rb * 768 + (((2 * k + h) ^ rb) & 15) * 16; }
  const int blk = (l >> 4) & 1, q = (l & 15) >> 2, p = l & 3;
  const char* tb = lb + (8 * h + q) * 128 + (32 * nt + 16 * blk + 8 * (p & 1) + 4 * (p >> 1)) * 2;
  asm volatile("s_waitcnt vmcnt(0)\n\ts_barrier" ::: "memory");
  STAMP(3);
  f32x16 acc = {};
  bf16x8 af[24], bf[24];
  auto rd = [&](auto I, auto ISB) {
    constexpr int ks = I.v;
    af[ks] = *(const bf16x8*)(la + aa[ks & 7] + (ks >> 3) * 256);
    if constexpr (ISB.v) bf[ks] = tr_frag(tb + ks * 2048, tb + ks * 2048 + 512);
    else bf[ks] = *(const bf16x8*)(lb + ab[ks & 7] + (ks >> 3) * 256);
  };
  auto gemm = [&](auto ISB) {
    sfor<0, 4>([&](auto I) { rd(I, ISB); });
    SB();
    sfor<0, 24>([&](auto I) {
      if constexpr (I.v + 4 < 24) rd(IC<I.v + 4>{}, ISB);
      acc = MFMA(af[I.v], bf[I.v], acc);
      SB();
    });
  };
  if (!isB) {
    gemm(IC<0>{});
    const float sc = 0.05103103630798288f * 1.4426950408889634f;
#pragma unroll
    for (int i = 0; i < 16; ++i) acc[i] *= sc;
  } else gemm(IC<1>{});
  STAMP(4);
  char* dst = ws + (size_t)prod * WBYTES;
  const int ntile = 2 * nb + nt, ctile = 2 * cb + ct;
#pragma unroll
  for (int s = 0; s < 2; ++s) {
    const int ks = 2 * ctile + s;
    const int idx = (prod < 2) ? ntile * 24 + ks : ((ntile / 3) * 24 + ks) * 3 + ntile % 3;
    gstore(dst + (size_t)idx * 1024 + l * 16, __builtin_bit_cast(u32x4, pack8(acc, s)));
  }
  STAMP(5);
  STOUT(ws + OFF_X1 + (size_t)TF * NTILE * TILEB + 2097152);
}

DI void partial_stats(const f32x2 (&v)[8], char* st, int r, int w, int h) {
  f32x2 s = v[0];
#pragma unroll
  for (int i = 1; i < 8; ++i) s += v[i];
  const float mean_w = xsum(s[0] + s[1]) * (1.f / 32.f);
  f32x2 q = {0.f, 0.f};
#pragma unroll
  for (int i = 0; i < 8; ++i) { const f32x2 d = v[i] - mean_w; q += d * d; }
  const float m2_w = xsum(q[0] + q[1]);
  *(float*)(st + r * 112 + h * 48 + w * 4) = h ? m2_w : mean_w;
}
DI void combine_stats(const char* st, int r, float& mean, float& rstd, float& vare) {
  f32x4 a[6];
#pragma unroll
  for (int i = 0; i < 6; ++i) a[i] = *(const f32x4*)(st + r * 112 + 16 * i);
  const f32x4 ms = a[0] + a[1] + a[2], qs = a[3] + a[4] + a[5];
  const float m = (ms[0] + ms[1] + ms[2] + ms[3]) * (1.f / 12.f);
  const f32x4 d0 = a[0] - m, d1 = a[1] - m, d2 = a[2] - m;
  const f32x4 e = qs + 32.f * (d0 * d0 + d1 * d1 + d2 * d2);
  mean = m;
  vare = (e[0] + e[1] + e[2] + e[3]) * (1.f / 384.f) + 1e-5f;
  rstd = __builtin_amdgcn_rsqf(vare);
}
template <int N> DI void wait2(u32x4 (&x)[2]) { asm volatile("s_waitcnt vmcnt(%2)" : "+v"(x[0]), "+v"(x[1]) : "i"(N)); }

__global__ __launch_bounds__(768, 3) void k1_temporal(const unsigned short* __restrict__ x, const char* __restrict__ WA,
                                                      const char* __restrict__ WB, char* __restrict__ H2, char* __restrict__ X1) {
  __shared__ __attribute__((aligned(16))) char lds[2 * TILEB + 2048 + 2 * 3584];
  char* himg = lds;
  char* qimg = lds + TILEB;
  char* amix = lds + 2 * TILEB;
  char* st1 = lds + 2 * TILEB + 2048;
  char* st2 = st1 + 3584;
  const int tid = threadIdx.x, w = __builtin_amdgcn_readfirstlane(tid >> 6), l = tid & 63, r = l & 31, h = l >> 5;
  const int pos = r & 3, t = r >> 2, n = 4 * blockIdx.x + pos;
  const bool valid = (r < 28) && (n < NP);
  STDECL; STAMP(0);
  asm volatile("" :: "s"(H2), "s"(X1), "s"(WB));
  bf16x8 wf[48];
  const unsigned voff = l * 16;
  const char* wa4 = WA + (size_t)w * 24 * 1024 + 4096;
  const char* wb4 = WB + (size_t)w * 24 * 1024 + 4096;
  auto wissue = [&](auto I) {
    constexpr int f = I.v;
    if constexpr (f < 24) wload<f, false>(wf[f], voff, wa4);
    else if constexpr (f < 48) wload<f - 24, false>(wf[f], voff, wb4);
  };
  const int so0 = slot_off(0, h, r), so1 = slot_off(1, h, r);
  u32x4 xr[2];
  {
#pragma unroll
    for (int i = 0; i < 2; ++i) {
      const int id = (2 * w + i) * 64 + l, row0 = id / 48, ps = id - 48 * row0;
      const int ch = (ps & ~15) | ((ps ^ row0) & 15);
      const int row = (row0 < 28) ? row0 : 27, nn = min(4 * (int)blockIdx.x + (row & 3), NP - 1);
      dma1p(x + ((size_t)(row >> 2) * NP + nn) * CH + 8 * ch, (unsigned)(size_t)qimg + (2 * w + i) * 1024);
    }
    sfor<0, 6>(wissue);
    wait_vm<6>();
    asm volatile("s_barrier" ::: "memory");
    STAMP(1);
    const char* xp = qimg + r * 768 + (w >> 2) * 256;
    xr[0] = *(const u32x4*)(xp + (((4 * w + h) ^ r) & 15) * 16); xr[1] = *(const u32x4*)(xp + (((4 * w + 2 + h) ^ r) & 15) * 16);
#pragma unroll
    for (int i = 0; i < 2; ++i) if (!valid) xr[i] = u32x4{0, 0, 0, 0};
    f32x2 v[8];
#pragma unroll
    for (int i = 0; i < 8; ++i) v[i] = unpk(xr[i >> 2][i & 3]);
    partial_stats(v, st1, r, w, h);
    SB();
    sfor<6, 10>(wissue);
    lds_barrier();
    float mean, rstd, vare;
    combine_stats(st1, r, mean, rstd, vare);
    const float nb = -mean * rstd;
#pragma unroll
    for (int i = 0; i < 2; ++i) {
      u32x4 p;
#pragma unroll
      for (int j = 0; j < 4; ++j) p[j] = pk(v[4 * i + j] * rstd + nb);
      *(u32x4*)(himg + (2 * w + i) * 1024 + (i ? so1 : so0)) = p;
    }
    SB();
    sfor<10, 14>(wissue);
    lds_barrier();
  }
  STAMP(2);
  f32x16 acc = {};
  {
    bf16x8 hf[24];
    hf[0] = *(const bf16x8*)(himg + so0); hf[1] = *(const bf16x8*)(himg + 1024 + so1);
    SB();
    sfor<0, 24>([&](auto I) {
      constexpr int ks = I.v;
      if constexpr (ks + 2 < 24) hf[ks + 2] = *(const bf16x8*)(himg + (ks + 2) * 1024 + ((ks & 1) ? so1 : so0));
      constexpr int iss = (ks < 10) ? 15 + ks : 24 + (ks - 9) / 2;
      if constexpr (ks < 10 || (ks & 1)) wissue(IC<iss - 1>{});
      wwait<iss - 1 - ks, false>(wf[ks]);
      acc = MFMA(wf[ks], hf[ks], acc);
      SB();
    });
  }
  STAMP(3);
  *(bf16x8*)(qimg + (2 * w) * 1024 + l * 16) = pack8(acc, 0);
  *(bf16x8*)(qimg + (2 * w + 1) * 1024 + l * 16) = pack8(acc, 1);
  lds_barrier();
  if (w == 0) {
    f32x16 sc;
#pragma unroll
    for (int i = 0; i < 16; ++i) {
      const int row = (i & 3) + 8 * (i >> 2) + 4 * h;
      sc[i] = ((i & 3) == pos && row < 28) ? 0.f : -3.0e38f;
    }
    bf16x8 ha[24], qb[24];
    sfor<0, 5>([&](auto I) {
      ha[I.v] = *(const bf16x8*)(himg + I.v * 1024 + ((I.v & 1) ? so1 : so0));
      qb[I.v] = *(const bf16x8*)(qimg + I.v * 1024 + l * 16);
    });
    SB();
    sfor<0, 24>([&](auto I) {
      constexpr int ks = I.v;
      if constexpr (ks + 5 < 24) {
        ha[ks + 5] = *(const bf16x8*)(himg + (ks + 5) * 1024 + (((ks + 5) & 1) ? so1 : so0));
        qb[ks + 5] = *(const bf16x8*)(qimg + (ks + 5) * 1024 + l * 16);
      }
      sc = MFMA(ha[ks], qb[ks], sc);
      SB();
    });
    float mx = sc[0];
#pragma unroll
    for (int i = 1; i < 16; ++i) mx = fmaxf(mx, sc[i]);
    mx = xmax(mx);
    float sum = 0.f;
#pragma unroll
    for (int i = 0; i < 16; ++i) { sc[i] = __builtin_amdgcn_exp2f(sc[i] - mx); sum += sc[i]; }
    const float inv = __builtin_amdgcn_rcpf(xsum(sum));
#pragma unroll
    for (int i = 0; i < 16; ++i) sc[i] *= inv;
    *(bf16x8*)(amix + l * 16) = pack8(sc, 0);
    *(bf16x8*)(amix + 1024 + l * 16) = pack8(sc, 1);
    lds_barrier();
  }
  SB();
  sfor<31, 44>(wissue);
  if (w != 0) lds_barrier();
  STAMP(4);
  {
    const int blk = (l >> 4) & 1, q = (l & 15) >> 2, p = l & 3, hh = p & 1;
    const int xr4 = 4 * (2 * blk + hh);
    const char* tb0 = himg + w * 2048 + blk * 1024 + (hh * 32 + ((4 * h + q) ^ xr4)) * 16 + 8 * (p >> 1);
    const char* tb1 = himg + w * 2048 + blk * 1024 + (hh * 32 + ((8 + 4 * h + q) ^ xr4)) * 16 + 8 * (p >> 1);
    f32x16 a = {};
    a = MFMA(tr_frag(tb0, tb1), *(const bf16x8*)(amix + l * 16), a);
    a = MFMA(tr_frag(tb0 + 256, tb1 + 256), *(const bf16x8*)(amix + 1024 + l * 16), a);
    *(bf16x8*)(qimg + (2 * w) * 1024 + l * 16) = pack8(a, 0);
    *(bf16x8*)(qimg + (2 * w + 1) * 1024 + l * 16) = pack8(a, 1);
  }
  lds_barrier();
  STAMP(5);
  acc = f32x16{};
  {
    bf16x8 hf[24];
    hf[0] = *(const bf16x8*)(qimg + l * 16); hf[1] = *(const bf16x8*)(qimg + 1024 + l * 16);
    SB();
    sfor<0, 24>([&](auto I) {
      constexpr int ks = I.v;
      if constexpr (ks + 2 < 24) hf[ks + 2] = *(const bf16x8*)(qimg + (ks + 2) * 1024 + l * 16);
      if constexpr (ks < 4) wissue(IC<44 + ks>{});
      wwait<((ks < 4) ? 45 + ks : 48) - 1 - (24 + ks), false>(wf[24 + ks]);
      acc = MFMA(wf[24 + ks], hf[ks], acc);
      SB();
    });
  }
  STAMP(6);
  {
    f32x2 v[8];
#pragma unroll
    for (int i = 0; i < 8; ++i) v[i] = unpk(pk(unpk(xr[i >> 2][i & 3]) + f32x2{acc[2 * i], acc[2 * i + 1]}));
    partial_stats(v, st2, r, w, h);
    const int rr = n & 31;
    const size_t tile = ((size_t)t * NTILE + (n >> 5)) * TILEB + (size_t)(2 * w) * 1024;
    const int d0 = slot_off(0, h, rr), d1 = slot_off(1, h, rr);
    lds_barrier();
    float mean, rstd, vare;
    combine_stats(st2, r, mean, rstd, vare);
    const float nb = -mean * rstd;
    if (r < 28) {
#pragma unroll
      for (int i = 0; i < 2; ++i) {
        u32x4 p;
#pragma unroll
        for (int j = 0; j < 4; ++j) p[j] = pk(v[4 * i + j] * rstd + nb);
        gstore(H2 + tile + i * 1024 + (i ? d1 : d0), p);
      }
      if (w == 0 && h == 0) *(f32x2*)(X1 + ((size_t)t * (32 * NTILE) + n) * 8) = f32x2{mean, vare * rstd};
    }
  }
  STAMP(7);
  STOUT(X1 + (size_t)TF * NTILE * TILEB);
}

constexpr bool w2a(int f) { return f < 16 || f >= 52; }
DI void mfma_s(f32x16& acc, const bf16x8& a, const bf16x8& b) {
  asm volatile("v_mfma_f32_32x32x16_bf16 %0, %1, %2, %0" : "+v"(acc) : "v"(a), "v"(b));
}
DI void mfma_s_end(f32x16& acc, const bf16x8& a, const bf16x8& b) {
  asm volatile("v_mfma_f32_32x32x16_bf16 %0, %1, %2, %0\n\ts_nop 7\n\ts_nop 7" : "+v"(acc) : "v"(a), "v"(b));
}
DI void mfma_s0(f32x16& acc, const bf16x8& a, const bf16x8& b) {
  asm volatile("s_nop 1\n\tv_mfma_f32_32x32x16_bf16 %0, %1, %2, 0" : "=&v"(acc) : "v"(a), "v"(b));
}
DI float max3(float a, float b, float c) { float r; asm("v_max3_f32 %0, %1, %2, %3" : "=v"(r) : "v"(a), "v"(b), "v"(c)); return r; }
constexpr int ev_slot(int e) { return e < 4 ? 2 * e + 1 : e < 20 ? 16 + 3 * ((e - 4) / 2) + ((e - 4) & 1) : 41 + 2 * (e - 20); }
constexpr int ev_at(int slot) { for (int e = 0; e < 24; ++e) if (ev_slot(e) == slot) return e; return -1; }
constexpr int ev_before(int slot) { int n = 0; for (int e = 0; e < 24; ++e) n += ev_slot(e) < slot; return n; }

__global__ __launch_bounds__(256, 1) void k2_spatial(const char* __restrict__ WA, const char* __restrict__ WB,
                                                     const char* __restrict__ H2, const char* __restrict__ X1,
                                                     unsigned short* __restrict__ out) {
  __shared__ __attribute__((aligned(16))) char lds[163840];
  const int t = blockIdx.x & 7, qt = blockIdx.x >> 3;
  if (t == 7) return;
  const int tid = threadIdx.x, w = __builtin_amdgcn_readfirstlane(tid >> 6), l = tid & 63, r = l & 31, h = l >> 5;
  asm volatile("" :: "s"(WB), "s"(X1), "s"(out));
  const unsigned voff = l * 16;
  const int so0 = slot_off(0, h, r), so1 = slot_off(1, h, r);
  unsigned vu[4];
  vu[0] = voff + (0 ^ w) * 2048 + 2048; vu[1] = voff + (1 ^ w) * 2048;
  vu[2] = voff + (2 ^ w) * 2048 + 2048; vu[3] = voff + (3 ^ w) * 2048;
  const char* keysm = H2 + (size_t)t * NTILE * TILEB - 2048;
  const int nt = (w < 3) ? 6 : 5;
  char* ring = lds + w * 40960;
  const unsigned ring0 = (unsigned)(size_t)ring;
  const char* wa4 = WA + (size_t)w * 72 * 1024 + 4096;
  const char* wb4 = WB + (size_t)w * 72 * 1024 + 4096;
  auto tile_of = [&](int jj) { const int i = 4 * jj + w + qt; return (i >= NTILE) ? i - NTILE : i; };
  auto dma_k = [&](auto K, const char* segsrc, unsigned m) {
    constexpr int k = K.v, cc = k >> 1;
    dma1<(k & 3) * 1024>(segsrc, m + (cc >= 2 ? 4096 : 0), vu[cc]);
  };
  u32x4 cb = {h ? 0u : 0xff610000u, 0u, 0u, 0u};
  auto sstart = [&](f32x16& sa, int tile) {
    const bool msk = (tile == NTILE - 1) && (r >= NP - 32 * (NTILE - 1));
    const u32x4 ca = {h ? 0u : (msk ? 0x3f803f80u : 0x3f80u), 0u, 0u, 0u};
    mfma_s0(sa, __builtin_bit_cast(bf16x8, ca), __builtin_bit_cast(bf16x8, cb));
  };

  STDECL; STAMP(0);
  bf16x8 qf[24];
  f32x16 scur, snext;
  bf16x8 kc[2];
  {
    constexpr int WIN = 30;
    bf16x8 wf[72];
    {
      const char* src = keysm + 2048 + (size_t)qt * TILEB + w * 6144;
      sfor<0, 4>([&](auto I) { dma1<I.v * 1024>(src, ring0 + 4 * 8192, voff); });
      sfor<0, 2>([&](auto I) { dma1<I.v * 1024>(src + 4096, ring0 + 4 * 8192 + 4096, voff); });
    }
    const char* src0 = keysm + (size_t)tile_of(0) * TILEB;
    const char* src1 = keysm + (size_t)tile_of(1) * TILEB;
    auto stream = [&](auto I) {
      if constexpr (I.v < 72) wload<I.v, true>(wf[I.v], voff, wa4);
      else if constexpr (I.v < 96) dma_k(IC<(I.v - 72) & 7>{}, src0 + ((I.v - 72) >> 3) * 8192, ring0 + ((I.v - 72) >> 3) * 8192);
      else dma_k(IC<(I.v - 96) & 7>{}, src1 + ((I.v - 96) >> 3) * 8192, ring0 + (3 + ((I.v - 96) >> 3)) * 8192);
    };
    sfor<0, 12>(stream);
    STAMP(1);
    wait_vm<12>();
    asm volatile("s_barrier" ::: "memory");
    sfor<12, WIN>(stream);
    STAMP(2);
    f32x16 acc[3] = {};
    bf16x8 hf[24];
    auto hread = [&](auto I) {
      hf[I.v] = *(const bf16x8*)(lds + (I.v / 6) * 40960 + 4 * 8192 + (I.v % 6) * 1024 + ((I.v & 1) ? so1 : so0));
    };
    sfor<0, 2>(hread);
    SB();
    sfor<0, 24>([&](auto I) {
      constexpr int ks = I.v;
      if constexpr (ks + 2 < 24) hread(IC<ks + 2>{});
      constexpr int i0 = (ks < 14) ? 3 * ks + WIN : 72 + 2 * (ks - 14), i1 = (ks < 14) ? i0 + 3 : i0 + 2;
      sfor<i0, i1>(stream);
      wwait<i1 - 3 * ks - 3, true>(wf[3 * ks + 2]); wtie<true>(wf[3 * ks]); wtie<true>(wf[3 * ks + 1]);
#pragma unroll
      for (int i = 0; i < 3; ++i) acc[i] = MFMA(wf[3 * ks + i], hf[ks], acc[i]);
      SB();
    });
    STAMP(3);
    sfor<0, 3>([&](auto I) {
#pragma unroll
      for (int s = 0; s < 2; ++s) *(bf16x8*)(ring + 3 * 8192 + (2 * I.v + s) * 1024 + l * 16) = pack8(acc[I.v], s);
      SB();
      stream(IC<92 + I.v>{});
    });
    lds_barrier();
    sfor<0, 4>([&](auto I) {
#pragma unroll
      for (int u = 3 * I.v; u < 3 * I.v + 3; ++u) {
        const int dt = (u & ~3) + ((u & 3) ^ w), ow = dt / 3;
        const char* p = lds + ow * 40960 + 3 * 8192 + (dt - 3 * ow) * 2048 + l * 16;
        qf[2 * u] = *(const bf16x8*)p; qf[2 * u + 1] = *(const bf16x8*)(p + 1024);
      }
      SB();
      if constexpr (I.v == 0) stream(IC<95>{});
    });
    lds_barrier();
    STAMP(4);
    bf16x8 kf[26];
    auto kread = [&](auto I) {
      constexpr int f = I.v;
      if constexpr ((f & 1) == 0) wait_vm<23 + ((f > 2) ? (f - 2) / 2 : 0) - (f + 1)>();
      kf[f] = *(const bf16x8*)(ring + f * 1024 + ((f & 1) ? so1 : so0));
    };
    sfor<0, 2>(kread);
    sstart(scur, tile_of(0));
    SB();
    sfor<0, 24>([&](auto I) {
      constexpr int f = I.v;
      kread(IC<f + 2>{});
      if constexpr (f & 1) stream(IC<96 + f / 2>{});
      if constexpr (f < 23) mfma_s(scur, kf[f], qf[f]); else mfma_s_end(scur, kf[f], qf[f]);
      SB();
    });
    kc[0] = kf[24]; kc[1] = kf[25];
  }

  const int blk = (l >> 4) & 1, q4 = (l & 15) >> 2, p4 = l & 3, hh = p4 & 1;
  const int xr4 = 4 * (2 * blk + hh);
  const int to0 = blk * 1024 + (hh * 32 + ((4 * h + q4) ^ xr4)) * 16 + 8 * (p4 >> 1);
  const int to1 = blk * 1024 + (hh * 32 + ((8 + 4 * h + q4) ^ xr4)) * 16 + 8 * (p4 >> 1);

  f32x16 o[12];
#pragma unroll
  for (int c = 0; c < 12; ++c) o[c] = f32x16{};
  float mrun = 0.f;
  f32x2 lsum = {0.f, 0.f};
  bf16x8 w2[72];
  auto w2issue = [&](auto I) { if constexpr (I.v < 72) wload<I.v, w2a(I.v)>(w2[I.v], voff, wb4); };

  auto period = [&](auto MODEC, int jj, int slot) {
    constexpr int MODE = MODEC.v;
    constexpr int PFD = 4;
    const int s1 = (slot + 1 >= 5) ? slot - 4 : slot + 1, s2 = (slot + 2 >= 5) ? slot - 3 : slot + 2;
    const int s3 = (slot + 3 >= 5) ? slot - 2 : slot + 3, s4 = (slot + 4 >= 5) ? slot - 1 : slot + 4;
    char* rb[3] = {ring + slot * 8192, ring + s1 * 8192, ring + s2 * 8192};
    char* nb[4] = {ring + s3 * 8192, ring + s4 * 8192, ring + slot * 8192, ring + s1 * 8192};
    const char* srcA = keysm + (size_t)tile_of(jj + 1) * TILEB;
    const char* srcB = keysm + (size_t)tile_of(jj + 2) * TILEB;
    auto stream = [&](auto S) {
      constexpr int e = ev_at(S.v);
      if constexpr (MODE == 2) {
        constexpr int sl = S.v, i0 = (sl < 8) ? sl : (sl < 24) ? 8 + (sl - 8) + (sl - 7) / 2 : 8 + sl;
        if constexpr (sl < 32) w2issue(IC<12 + i0>{});
        if constexpr (sl >= 8 && sl < 24 && (sl & 1) == 0) w2issue(IC<13 + i0>{});
      } else if constexpr (e >= 0) {
        if constexpr (e < 4) dma_k(IC<4 + e>{}, srcA + 8192, ring0 + s4 * 8192);
        else if constexpr (e < 12) dma_k(IC<e - 4>{}, srcA + 16384, ring0 + slot * 8192);
        else if constexpr (MODE == 1) w2issue(IC<e - 12>{});
        else if constexpr (e < 20) dma_k(IC<e - 12>{}, srcB, ring0 + s1 * 8192);
        else dma_k(IC<e - 20>{}, srcB + 8192, ring0 + s2 * 8192);
      }
    };
    bf16x8 kf[26], tf[24];
    kf[0] = kc[0]; kf[1] = kc[1];
    auto kread = [&](auto I, auto S) {
      constexpr int f = I.v;
      if constexpr (MODE == 0 || (MODE == 1 && f < 24)) {
        if constexpr ((f & 1) == 0) wait_vm<ev_before(S.v) - 1 - (f + 1 - 12)>();
        kf[f] = *(const bf16x8*)(nb[f >> 3] + (f & 7) * 1024 + ((f & 1) ? so1 : so0));
      }
    };
    auto tread = [&](auto I) {
      constexpr int u = I.v >> 1;
      const char* b = rb[u >> 2] + (u & 3) * 2048 + (I.v & 1) * 256;
      tf[I.v] = tr_frag(b + to0, b + to1);
    };
    float mx;
    if constexpr (MODE != 2) sstart(snext, tile_of(jj + 1));
    sfor<0, 2>([&](auto I) {
      kread(IC<I.v + 2>{}, I);
      if constexpr (MODE != 2) mfma_s(snext, kf[I.v], qf[I.v]);
      if constexpr (I.v == 0) mx = max3(max3(max3(max3(scur[0], scur[1], scur[2]), scur[3], scur[4]), scur[5], scur[6]), scur[7], scur[8]);
      if constexpr (I.v == 1) {
        mx = max3(max3(max3(max3(mx, scur[9], scur[10]), scur[11], scur[12]), scur[13], scur[14]), scur[15], scur[15]);
        const unsigned u = __builtin_bit_cast(unsigned, mx);
        const auto sw = __builtin_amdgcn_permlane32_swap(u, u, false, false);
        mx = max3(__builtin_bit_cast(float, (unsigned)sw[0]), __builtin_bit_cast(float, (unsigned)sw[1]), mx);
      }
      stream(I);
      SB();
    });
    if (jj == 0 || __any(mx > 64.f)) {
      const unsigned mb = pack2(mrun + ((jj == 0) ? mx : fmaxf(mx, 0.f)), 0.f) & 0xffffu;
      const float dl = bflo(mb) - mrun;
      mrun = bflo(mb);
      cb[0] = h ? 0u : (0xff610000u | (mb ^ 0x8000u));
#pragma unroll
      for (int i = 0; i < 16; ++i) scur[i] -= dl;
      if constexpr (MODE != 2) {
        asm volatile("s_nop 7\n\ts_nop 7\n\tv_sub_f32 %0, %0, %1" : "+v"(snext[0]) : "v"(dl));
#pragma unroll
        for (int i = 1; i < 15; ++i) asm volatile("v_sub_f32 %0, %0, %1" : "+v"(snext[i]) : "v"(dl));
        asm volatile("v_sub_f32 %0, %0, %1\n\ts_nop 1" : "+v"(snext[15]) : "v"(dl));
      }
      if (jj != 0) {
        const float f = __builtin_amdgcn_exp2f(-dl);
        lsum *= f;
#pragma unroll
        for (int c = 0; c < 12; ++c)
#pragma unroll
          for (int i = 0; i < 16; ++i) {
            float tmp;
            asm volatile("v_accvgpr_read_b32 %1, %0\n\tv_mul_f32 %1, %1, %2\n\tv_accvgpr_write_b32 %0, %1" : "+a"(o[c][i]), "=&v"(tmp) : "v"(f));
          }
        asm volatile("s_nop 4");
      }
    }
    SB();
    bf16x8 pf0, pf1;
    sfor<2, 8>([&](auto I) {
      constexpr int f = I.v;
      if constexpr (f + 2 < 8) kread(IC<f + 2>{}, I);
      if constexpr (f >= 4) tread(IC<f - 4>{});
      if constexpr (MODE != 2) mfma_s(snext, kf[f], qf[f]);
      if constexpr (f < 6) {
#pragma unroll
        for (int i = 3 * (f - 2); i < 3 * (f - 2) + 3; ++i) scur[i] = __builtin_amdgcn_exp2f(scur[i]);
      } else {
        scur[2 * f] = __builtin_amdgcn_exp2f(scur[2 * f]); scur[2 * f + 1] = __builtin_amdgcn_exp2f(scur[2 * f + 1]);
        if constexpr (f == 6) pf0 = pack8(scur, 0);
      }
      stream(I);
      SB();
    });
    auto oput = [&](auto I) {
      constexpr int u = I.v, k = 3 * (u >> 2) + (u & 3) - 1;
      if constexpr ((u & 3) != 0) {
        char* dst = ((k < 4) ? nb[k >> 1] : rb[(k - 4) >> 1]) + (k & 1) * 4096 + l * 16;
#pragma unroll
        for (int g = 0; g < 4; ++g) *(f32x4*)(dst + g * 1024) = f32x4{o[u][4 * g], o[u][4 * g + 1], o[u][4 * g + 2], o[u][4 * g + 3]};
      }
    };
    sfor<0, 24>([&](auto I) {
      constexpr int p = I.v;
      if constexpr (p + PFD < 24) tread(IC<p + PFD>{});
      if constexpr (p >= 22) kread(IC<p - 14>{}, IC<8 + p>{});
      if constexpr (p == 0) pf1 = pack8(scur, 1);
      o[p >> 1] = MFMA(tf[p], (p & 1) ? pf1 : pf0, o[p >> 1]);
      if constexpr (p >= 2 && p < 10) lsum += f32x2{scur[2 * (p - 2)], scur[2 * (p - 2) + 1]};
      stream(IC<8 + p>{});
      if constexpr (MODE == 2 && p >= 5 && (p & 1)) oput(IC<(p - 3) / 2>{});
      SB();
    });
    if constexpr (MODE == 2) oput(IC<11>{});
    else {
      sfor<8, 24>([&](auto I) {
        constexpr int f = I.v;
        kread(IC<f + 2>{}, IC<24 + f>{});
        if constexpr (f < 23) { mfma_s(snext, kf[f], qf[f]); stream(IC<24 + f>{}); }
        else { stream(IC<24 + f>{}); mfma_s_end(snext, kf[f], qf[f]); }
        SB();
      });
      if constexpr (MODE == 0) { kc[0] = kf[24]; kc[1] = kf[25]; }
      scur = snext;
    }
  };
  int slot = 0;
  for (int jj = 0; jj < nt - 2; ++jj) {
    period(IC<0>{}, jj, slot);
    slot = (slot + 3 >= 5) ? slot - 2 : slot + 3;
  }
  STAMP(5);
  period(IC<1>{}, nt - 2, slot);
  slot = (slot + 3 >= 5) ? slot - 2 : slot + 3;
  STAMP(6);
  period(IC<2>{}, nt - 1, slot);
  STAMP(7);

  const float ltot = xsum(lsum[0] + lsum[1]);
  auto oslot = [&](int sw, int k) {
    const int b = (sw < 3) ? 0 : 4;
    const int i = b + k + 6;
    return lds + sw * 40960 + ((i >= 10) ? i - 10 : i) * 4096;
  };
  if (h == 0) *(f32x2*)(oslot(w, 9) + r * 8) = f32x2{mrun, ltot};
  SB();
  sfor<52, 54>(w2issue);
  lds_barrier();
  float fs[4];
  {
    const f32x2 a = *(const f32x2*)(oslot(w, 9) + r * 8), b = *(const f32x2*)(oslot(w ^ 1, 9) + r * 8);
    const f32x2 c = *(const f32x2*)(oslot(w ^ 2, 9) + r * 8), d = *(const f32x2*)(oslot(w ^ 3, 9) + r * 8);
    const float m = fmaxf(fmaxf(a[0], b[0]), fmaxf(c[0], d[0]));
    fs[0] = __builtin_amdgcn_exp2f(a[0] - m); fs[1] = __builtin_amdgcn_exp2f(b[0] - m);
    fs[2] = __builtin_amdgcn_exp2f(c[0] - m); fs[3] = __builtin_amdgcn_exp2f(d[0] - m);
    const float inv = __builtin_amdgcn_rcpf(a[1] * fs[0] + b[1] * fs[1] + c[1] * fs[2] + d[1] * fs[3]);
    fs[0] *= inv; fs[1] *= inv; fs[2] *= inv; fs[3] *= inv;
  }
  f32x4 pa[4][3];
  u32x4 opk[2];
  auto mread = [&](auto SG, auto G) {
#pragma unroll
    for (int cc = 1; cc < 4; ++cc) pa[G.v][cc - 1] = *(const f32x4*)(oslot(w ^ cc, 3 * SG.v + cc - 1) + l * 16 + G.v * 1024);
  };
  auto mstep = [&](auto SG, auto G) {
    constexpr int sg = SG.v, g = G.v;
    f32x4 own;
#pragma unroll
    for (int e = 0; e < 4; ++e) {
      float tmp;
      asm volatile("v_accvgpr_read_b32 %0, %1" : "=v"(tmp) : "a"(o[4 * sg][4 * g + e]));
      own[e] = tmp;
    }
    own = own * fs[0] + pa[g][0] * fs[1] + pa[g][1] * fs[2] + pa[g][2] * fs[3];
    opk[g >> 1][2 * (g & 1)] = pack2(own[0], own[1]); opk[g >> 1][2 * (g & 1) + 1] = pack2(own[2], own[3]);
    if constexpr (g == 3) {
      char* dst = oslot(w ^ 1, 3 * sg) + l * 16;
      *(u32x4*)dst = opk[0]; *(u32x4*)(dst + 1024) = opk[1];
    }
  };
  sfor<0, 4>([&](auto G) { mread(IC<0>{}, G); });
  SB();
  sfor<54, 56>(w2issue);
  sfor<0, 4>([&](auto G) { mstep(IC<0>{}, G); });
  lds_barrier();
  STAMP(8);
  f32x16 acc[3] = {};
  u32x4 xv[6];
  f32x2 ms;
  {
    const char* x1 = H2 + ((size_t)t * NTILE + qt) * TILEB + (size_t)(6 * w) * 1024 + 2048;
    const char* xa = x1 + so0;
    const char* xb = x1 + so1;
    bf16x8 of[24];
    auto oread = [&](auto I) {
      constexpr int ks = I.v;
      of[ks] = *(const bf16x8*)(oslot(((ks >> 1) & 3) ^ 1, 3 * (ks >> 3)) + (ks & 1) * 1024 + l * 16);
    };
    sfor<0, 3>([&](auto SG) {
      constexpr int sg = SG.v;
      sfor<8 * sg, 8 * sg + 2>(oread);
      SB();
      sfor<0, 8>([&](auto K) {
        constexpr int k = K.v, ks = 8 * sg + k;
        if constexpr (k + 2 < 8) oread(IC<ks + 2>{});
        if constexpr (sg < 2 && k < 4) mread(IC<sg + 1>{}, K);
        if constexpr (ks < 8) sfor<56 + 2 * ks, 58 + 2 * ks>(w2issue);
        else if constexpr (ks < 14) gload_p<(ks - 8) * 1024 - 2048>(xv[ks - 8], ((ks - 8) & 1) ? xb : xa);
        else if constexpr (ks == 14)
          asm volatile("global_load_dwordx2 %0, %1, off" : "=v"(ms) : "v"(X1 + ((size_t)t * (32 * NTILE) + 32 * qt + r) * 8));
        constexpr int issued = (ks < 8) ? 58 + 2 * ks : ((ks < 15) ? 72 + (ks - 7) : 79);
        wwait<issued - 3 * ks - 3, w2a(3 * ks + 2)>(w2[3 * ks + 2]); wtie<w2a(3 * ks)>(w2[3 * ks]); wtie<w2a(3 * ks + 1)>(w2[3 * ks + 1]);
#pragma unroll
        for (int i = 0; i < 3; ++i) acc[i] = MFMA(w2[3 * ks + i], of[ks], acc[i]);
        if constexpr (sg < 2 && k >= 2 && k < 6) mstep(IC<sg + 1>{}, IC<k - 2>{});
        SB();
      });
      if constexpr (sg < 2) lds_barrier();
    });
  }
  STAMP(9);
  wait6<0>(xv);
  asm volatile("" : "+v"(ms));
  {
    char* ob0 = oslot(w ^ 2, 1);
    char* ob1 = oslot(w ^ 3, 2) - 16 * 208;
#pragma unroll
    for (int i = 0; i < 6; ++i) {
      u32x4 p;
#pragma unroll
      for (int j = 0; j < 4; ++j)
        p[j] = pk(unpk(xv[i][j]) * ms[1] + (ms[0] + f32x2{acc[i >> 1][8 * (i & 1) + 2 * j], acc[i >> 1][8 * (i & 1) + 2 * j + 1]}));
      *(u32x4*)(((r < 16) ? ob0 : ob1) + r * 208 + i * 32 + h * 16) = p;
    }
    unsigned short* obase = out + ((size_t)t * NP + 32 * qt) * CH + 96 * w;
#pragma unroll
    for (int i = 0; i < 6; ++i) {
      const int id = i * 64 + l, row = id / 12, ch = id - 12 * row;
      const u32x4 p = *(const u32x4*)(((i < 3) ? ob0 : ob1) + row * 208 + ch * 16);
      if (32 * qt + row < NP) *(u32x4*)(obase + (size_t)row * CH + 8 * ch) = p;
    }
  }
  STAMP(10);
  STOUT((char*)X1 + (size_t)TF * NTILE * TILEB + 1048576);
  asm volatile(".Lcode_end:");
}

extern "C" void kernel_launch(void* const* d_in, const int* in_sizes, int n_in,
                              void* d_out, int out_size, void* d_ws, size_t ws_size,
                              hipStream_t stream) {
  const unsigned short* x = (const unsigned short*)d_in[0];
  const unsigned short* wqkv_t = (const unsigned short*)d_in[1];
  const unsigned short* wo_t = (const unsigned short*)d_in[2];
  const unsigned short* wqkv_s = (const unsigned short*)d_in[3];
  const unsigned short* wo_s = (const unsigned short*)d_in[4];
  char* ws = (char*)d_ws;
  k0_fold<<<256, 256, 0, stream>>>(wqkv_t, wo_t, wqkv_s, wo_s, ws, x);
  k1_temporal<<<184, 768, 0, stream>>>(x, ws + OFF_WA_T, ws + OFF_WB_T, ws + OFF_H2, ws + OFF_X1);
  k2_spatial<<<184, 256, 0, stream>>>(ws + OFF_WA_S, ws + OFF_WB_S, ws + OFF_H2, ws + OFF_X1, (unsigned short*)d_out);
}
```

```cpp
#include <hip/hip_runtime.h>
#include <hip/hip_bf16.h>

using bf16x8 = __attribute__((ext_vector_type(8))) __bf16;
using bf16x2 = __attribute__((ext_vector_type(2))) __bf16;
using s16x4  = __attribute__((ext_vector_type(4))) short;
using s16x8  = __attribute__((ext_vector_type(8))) short;
using f32x16 = __attribute__((ext_vector_type(16))) float;
using f32x4  = __attribute__((ext_vector_type(4))) float;
using f32x2  = __attribute__((ext_vector_type(2))) float;
using u32x4  = __attribute__((ext_vector_type(4))) unsigned;
typedef __attribute__((address_space(3))) s16x4 lds_s16x4;

#define DI __device__ __forceinline__
#define MFMA(a, b, c) __builtin_amdgcn_mfma_f32_32x32x16_bf16((a), (b), (c), 0, 0, 0)

constexpr int TF = 7, NP = 729, CH = 384;
constexpr int NTILE = 23;
constexpr int WBYTES = 12 * 24 * 1024;
constexpr int TILEB = 24 * 1024;
constexpr size_t OFF_WA_T = 0, OFF_WB_T = WBYTES, OFF_WA_S = 2 * WBYTES, OFF_WB_S = 3 * WBYTES;
constexpr size_t OFF_H2 = 4 * (size_t)WBYTES;
constexpr size_t OFF_X1 = OFF_H2 + (size_t)TF * NTILE * TILEB;

DI int perm23(int r) { return (r & 0x13) | ((r & 4) << 1) | ((r & 8) >> 1); }
DI int slot_off(int ksodd, int h, int r) { return (h * 32 + (r ^ (4 * (2 * ksodd + h)))) * 16; }

DI unsigned pack2(float a, float b) {
  f32x2 p = {a, b};
  return __builtin_bit_cast(unsigned, __builtin_convertvector(p, bf16x2));
}
DI bf16x8 pack8(const f32x16& x, int s) {
  u32x4 p;
  p[0] = pack2(x[8 * s + 0], x[8 * s + 1]); p[1] = pack2(x[8 * s + 2], x[8 * s + 3]);
  p[2] = pack2(x[8 * s + 4], x[8 * s + 5]); p[3] = pack2(x[8 * s + 6], x[8 * s + 7]);
  return __builtin_bit_cast(bf16x8, p);
}
DI float bflo(unsigned u) { return __builtin_bit_cast(float, u << 16); }
DI float bfhi(unsigned u) { return __builtin_bit_cast(float, u & 0xffff0000u); }
DI f32x2 unpk(unsigned u) { return f32x2{bflo(u), bfhi(u)}; }
DI unsigned pk(f32x2 p) { return __builtin_bit_cast(unsigned, __builtin_convertvector(p, bf16x2)); }
DI float xsum(float v) {
  const unsigned u = __builtin_bit_cast(unsigned, v);
  const auto r = __builtin_amdgcn_permlane32_swap(u, u, false, false);
  return __builtin_bit_cast(float, (unsigned)r[0]) + __builtin_bit_cast(float, (unsigned)r[1]);
}
DI float xmax(float v) {
  const unsigned u = __builtin_bit_cast(unsigned, v);
  const auto r = __builtin_amdgcn_permlane32_swap(u, u, false, false);
  return fmaxf(__builtin_bit_cast(float, (unsigned)r[0]), __builtin_bit_cast(float, (unsigned)r[1]));
}
DI void lds_barrier() { asm volatile("s_waitcnt lgkmcnt(0)\n\ts_barrier" ::: "memory"); }
#define SB() __builtin_amdgcn_sched_barrier(0)
#define STDECL
#define STAMP(i)
#define STACC(i, prev)
#define STOUT(buf)
template <int I> struct IC { static constexpr int v = I; };
template <int B, int E, class F> DI void sfor(F&& f) { if constexpr (B < E) { f(IC<B>{}); sfor<B + 1, E>(f); } }
template <int OFF> DI void gload_a(bf16x8& dst, unsigned voff, const char* sbase) {
  asm volatile("global_load_dwordx4 %0, %1, %2 offset:%3" : "=a"(dst) : "v"(voff), "s"(sbase), "i"(OFF));
}
template <int OFF> DI void gload_v(bf16x8& dst, unsigned voff, const char* sbase) {
  asm volatile("global_load_dwordx4 %0, %1, %2 offset:%3" : "=v"(dst) : "v"(voff), "s"(sbase), "i"(OFF));
}
template <int OFF> DI void gload_p(u32x4& dst, const void* p) {
  asm volatile("global_load_dwordx4 %0, %1, off offset:%2" : "=v"(dst) : "v"(p), "i"(OFF));
}
template <int IDX, bool AG> DI void wload(bf16x8& dst, unsigned voff, const char* base4k) {
  if constexpr (AG) gload_a<(IDX % 8) * 1024 - 4096>(dst, voff, base4k + (IDX / 8) * 8192);
  else gload_v<(IDX % 8) * 1024 - 4096>(dst, voff, base4k + (IDX / 8) * 8192);
}
template <int N, bool AG> DI void wwait(bf16x8& a) {
  constexpr int M = (N > 63) ? 63 : N;
  if constexpr (AG) asm volatile("s_waitcnt vmcnt(%1)" : "+a"(a) : "i"(M));
  else asm volatile("s_waitcnt vmcnt(%1)" : "+v"(a) : "i"(M));
}
template <bool AG> DI void wtie(bf16x8& a) {
  if constexpr (AG) asm volatile("" : "+a"(a)); else asm volatile("" : "+v"(a));
}
template <int N> DI void wait6(u32x4 (&x)[6]) {
  asm volatile("s_waitcnt vmcnt(%6)" : "+v"(x[0]), "+v"(x[1]), "+v"(x[2]), "+v"(x[3]), "+v"(x[4]), "+v"(x[5]) : "i"(N));
}

DI bf16x8 tr_frag(const char* p0, const char* p1) {
  s16x4 lo = __builtin_amdgcn_ds_read_tr16_b64_v4i16((lds_s16x4*)p0);
  s16x4 hi = __builtin_amdgcn_ds_read_tr16_b64_v4i16((lds_s16x4*)p1);
  s16x8 v = __builtin_shufflevector(lo, hi, 0, 1, 2, 3, 4, 5, 6, 7);
  return __builtin_bit_cast(bf16x8, v);
}

template <int IMM> DI void dma1(const char* srcm, unsigned m0v, unsigned voff) {
  asm volatile("s_mov_b32 m0, %1\n\ts_nop 0\n\tglobal_load_lds_dwordx4 %2, %0 offset:%3"
               :: "s"(srcm), "s"(m0v), "v"(voff), "i"(IMM) : "memory");
}
template <int N> DI void wait_vm() { asm volatile("s_waitcnt vmcnt(%0)" :: "i"(N) : "memory"); }

DI void gstore(void* p, u32x4 v) {
  asm volatile("global_store_dwordx4 %0, %1, off sc1\n\ts_nop 1" :: "v"(p), "v"(v) : "memory");
}
DI void dma1p(const void* p, unsigned m0v) {
  asm volatile("s_mov_b32 m0, %1\n\ts_nop 0\n\tglobal_load_lds_dwordx4 %0, off" :: "v"(p), "s"(m0v) : "memory");
}

__global__ __launch_bounds__(256) void k0_fold(const unsigned short* __restrict__ wqkv_t, const unsigned short* __restrict__ wo_t,
                                               const unsigned short* __restrict__ wqkv_s, const unsigned short* __restrict__ wo_s,
                                               char* __restrict__ ws) {
  __shared__ __attribute__((aligned(16))) char lds[2 * 49152];
  char* la = lds;
  char* lb = lds + 49152;
  const int b = blockIdx.x, prod = b / 36, rem = b % 36, nb = rem / 6, cb = rem % 6;
  const int tid = threadIdx.x, w = __builtin_amdgcn_readfirstlane(tid >> 6), l = tid & 63, r = l & 31, h = l >> 5;
  const unsigned short* wqkv = (prod < 2) ? wqkv_t : wqkv_s;
  const unsigned short* wo = (prod < 2) ? wo_t : wo_s;
  const bool isB = prod & 1;
  STDECL; STAMP(0);
  {
    const unsigned short* asrc = wqkv + (size_t)(64 * cb + 16 * w) * 1152 + (isB ? 768 : 0);
    const unsigned short* bsrc = wqkv + (size_t)(64 * nb + 16 * w) * 1152 + 384;
    const unsigned short* osrc = wo + (size_t)(96 * w + (l >> 3)) * 384 + 64 * nb + 8 * (l & 7);
    const unsigned la0 = (unsigned)(size_t)la + w * 12288, lb0 = (unsigned)(size_t)lb + w * 12288;
#pragma unroll
    for (int j = 0; j < 3; ++j) {
      const int id = 64 * j + l, row = id / 48, pos = id - 48 * row;
#pragma unroll
      for (int g = 0; g < 4; ++g) {
        const int rw = 4 * g + row;
        const int off = rw * 1152 + 8 * ((pos & ~15) | ((pos ^ rw) & 15));
        dma1p(asrc + off, la0 + (3 * g + j) * 1024);
        if (!isB) dma1p(bsrc + off, lb0 + (3 * g + j) * 1024);
        else dma1p(osrc + (size_t)(8 * (3 * g + j)) * 384, lb0 + (3 * g + j) * 1024);
      }
    }
  }
  STAMP(1);
  const int ct = w & 1, nt = w >> 1;
  const int ra = 32 * ct + perm23(r), rb = 32 * nt + perm23(r);
  int aa[8], ab[8];
#pragma unroll
  for (int k = 0; k < 8; ++k) { aa[k] = ra * 768 + (((2 * k + h) ^ ra) & 15) * 16; ab[k] = rb * 768 + (((2 * k + h) ^ rb) & 15) * 16; }
  const int blk = (l >> 4) & 1, q = (l & 15) >> 2, p = l & 3;
  const char* tb = lb + (8 * h + q) * 128 + (32 * nt + 16 * blk + 8 * (p & 1) + 4 * (p >> 1)) * 2;
  asm volatile("s_waitcnt vmcnt(0)\n\ts_barrier" ::: "memory");
  STAMP(3);
  f32x16 acc = {};
  bf16x8 af[24], bf[24];
  auto rd = [&](auto I, auto ISB) {
    constexpr int ks = I.v;
    af[ks] = *(const bf16x8*)(la + aa[ks & 7] + (ks >> 3) * 256);
    if constexpr (ISB.v) bf[ks] = tr_frag(tb + ks * 2048, tb + ks * 2048 + 512);
    else bf[ks] = *(const bf16x8*)(lb + ab[ks & 7] + (ks >> 3) * 256);
  };
  auto gemm = [&](auto ISB) {
    sfor<0, 4>([&](auto I) { rd(I, ISB); });
    SB();
    sfor<0, 24>([&](auto I) {
      if constexpr (I.v + 4 < 24) rd(IC<I.v + 4>{}, ISB);
      acc = MFMA(af[I.v], bf[I.v], acc);
      SB();
    });
  };
  if (!isB) {
    gemm(IC<0>{});
    const float sc = 0.05103103630798288f * 1.4426950408889634f;
#pragma unroll
    for (int i = 0; i < 16; ++i) acc[i] *= sc;
  } else gemm(IC<1>{});
  STAMP(4);
  char* dst = ws + (size_t)prod * WBYTES;
  const int ntile = 2 * nb + nt, ctile = 2 * cb + ct;
#pragma unroll
  for (int s = 0; s < 2; ++s) {
    const int ks = 2 * ctile + s;
    const int idx = (prod < 2) ? ntile * 24 + ks : ((ntile / 3) * 24 + ks) * 3 + ntile % 3;
    gstore(dst + (size_t)idx * 1024 + l * 16, __builtin_bit_cast(u32x4, pack8(acc, s)));
  }
  STAMP(5);
  STOUT(ws + OFF_X1 + (size_t)TF * NTILE * TILEB + 2097152);
}

DI void partial_stats(const f32x2 (&v)[8], char* st, int r, int w, int h) {
  f32x2 s = v[0];
#pragma unroll
  for (int i = 1; i < 8; ++i) s += v[i];
  const float mean_w = xsum(s[0] + s[1]) * (1.f / 32.f);
  f32x2 q = {0.f, 0.f};
#pragma unroll
  for (int i = 0; i < 8; ++i) { const f32x2 d = v[i] - mean_w; q += d * d; }
  const float m2_w = xsum(q[0] + q[1]);
  *(float*)(st + r * 112 + h * 48 + w * 4) = h ? m2_w : mean_w;
}
DI void combine_stats(const char* st, int r, float& mean, float& rstd, float& vare) {
  f32x4 a[6];
#pragma unroll
  for (int i = 0; i < 6; ++i) a[i] = *(const f32x4*)(st + r * 112 + 16 * i);
  const f32x4 ms = a[0] + a[1] + a[2], qs = a[3] + a[4] + a[5];
  const float m = (ms[0] + ms[1] + ms[2] + ms[3]) * (1.f / 12.f);
  const f32x4 d0 = a[0] - m, d1 = a[1] - m, d2 = a[2] - m;
  const f32x4 e = qs + 32.f * (d0 * d0 + d1 * d1 + d2 * d2);
  mean = m;
  vare = (e[0] + e[1] + e[2] + e[3]) * (1.f / 384.f) + 1e-5f;
  rstd = __builtin_amdgcn_rsqf(vare);
}
template <int N> DI void wait2(u32x4 (&x)[2]) { asm volatile("s_waitcnt vmcnt(%2)" : "+v"(x[0]), "+v"(x[1]) : "i"(N)); }

__global__ __launch_bounds__(768, 3) void k1_temporal(const unsigned short* __restrict__ x, const char* __restrict__ WA,
                                                      const char* __restrict__ WB, char* __restrict__ H2, char* __restrict__ X1) {
  __shared__ __attribute__((aligned(16))) char lds[2 * TILEB + 2048 + 2 * 3584];
  char* himg = lds;
  char* qimg = lds + TILEB;
  char* amix = lds + 2 * TILEB;
  char* st1 = lds + 2 * TILEB + 2048;
  char* st2 = st1 + 3584;
  const int tid = threadIdx.x, w = __builtin_amdgcn_readfirstlane(tid >> 6), l = tid & 63, r = l & 31, h = l >> 5;
  const int pos = r & 3, t = r >> 2, n = 4 * blockIdx.x + pos;
  const bool valid = (r < 28) && (n < NP);
  STDECL; STAMP(0);
  asm volatile("" :: "s"(H2), "s"(X1), "s"(WB));
  bf16x8 wf[48];
  const unsigned voff = l * 16;
  const char* wa4 = WA + (size_t)w * 24 * 1024 + 4096;
  const char* wb4 = WB + (size_t)w * 24 * 1024 + 4096;
  auto wissue = [&](auto I) {
    constexpr int f = I.v;
    if constexpr (f < 24) wload<f, false>(wf[f], voff, wa4);
    else if constexpr (f < 48) wload<f - 24, false>(wf[f], voff, wb4);
  };
  const int so0 = slot_off(0, h, r), so1 = slot_off(1, h, r);
  u32x4 xr[2];
  {
#pragma unroll
    for (int i = 0; i < 2; ++i) {
      const int id = (2 * w + i) * 64 + l, row0 = id / 48, ps = id - 48 * row0;
      const int ch = (ps & ~15) | ((ps ^ row0) & 15);
      const int row = (row0 < 28) ? row0 : 27, nn = min(4 * (int)blockIdx.x + (row & 3), NP - 1);
      dma1p(x + ((size_t)(row >> 2) * NP + nn) * CH + 8 * ch, (unsigned)(size_t)qimg + (2 * w + i) * 1024);
    }
    asm volatile("s_barrier" ::: "memory");
    sfor<0, 4>(wissue);
    wait_vm<4>();
    asm volatile("s_barrier" ::: "memory");
    STAMP(1);
    const char* xp = qimg + r * 768 + (w >> 2) * 256;
    xr[0] = *(const u32x4*)(xp + (((4 * w + h) ^ r) & 15) * 16); xr[1] = *(const u32x4*)(xp + (((4 * w + 2 + h) ^ r) & 15) * 16);
#pragma unroll
    for (int i = 0; i < 2; ++i) if (!valid) xr[i] = u32x4{0, 0, 0, 0};
    f32x2 v[8];
#pragma unroll
    for (int i = 0; i < 8; ++i) v[i] = unpk(xr[i >> 2][i & 3]);
    partial_stats(v, st1, r, w, h);
    SB();
    sfor<4, 9>(wissue);
    lds_barrier();
    float mean, rstd, vare;
    combine_stats(st1, r, mean, rstd, vare);
    const float nb = -mean * rstd;
#pragma unroll
    for (int i = 0; i < 2; ++i) {
      u32x4 p;
#pragma unroll
      for (int j = 0; j < 4; ++j) p[j] = pk(v[4 * i + j] * rstd + nb);
      *(u32x4*)(himg + (2 * w + i) * 1024 + (i ? so1 : so0)) = p;
    }
    SB();
    sfor<9, 14>(wissue);
    lds_barrier();
  }
  STAMP(2);
  f32x16 acc = {};
  {
    bf16x8 hf[24];
    hf[0] = *(const bf16x8*)(himg + so0); hf[1] = *(const bf16x8*)(himg + 1024 + so1);
    SB();
    sfor<0, 24>([&](auto I) {
      constexpr int ks = I.v;
      if constexpr (ks + 2 < 24) hf[ks + 2] = *(const bf16x8*)(himg + (ks + 2) * 1024 + ((ks & 1) ? so1 : so0));
      constexpr int iss = (ks < 10) ? 15 + ks : 24;
      if constexpr (ks < 10) wissue(IC<iss - 1>{});
      wwait<iss - 1 - ks, false>(wf[ks]);
      acc = MFMA(wf[ks], hf[ks], acc);
      SB();
    });
  }
  STAMP(3);
  *(bf16x8*)(qimg + (2 * w) * 1024 + l * 16) = pack8(acc, 0);
  *(bf16x8*)(qimg + (2 * w + 1) * 1024 + l * 16) = pack8(acc, 1);
  lds_barrier();
  if (w == 0) {
    f32x16 sc;
#pragma unroll
    for (int i = 0; i < 16; ++i) {
      const int row = (i & 3) + 8 * (i >> 2) + 4 * h;
      sc[i] = ((i & 3) == pos && row < 28) ? 0.f : -3.0e38f;
    }
    bf16x8 ha[24], qb[24];
    sfor<0, 5>([&](auto I) {
      ha[I.v] = *(const bf16x8*)(himg + I.v * 1024 + ((I.v & 1) ? so1 : so0));
      qb[I.v] = *(const bf16x8*)(qimg + I.v * 1024 + l * 16);
    });
    SB();
    sfor<0, 24>([&](auto I) {
      constexpr int ks = I.v;
      if constexpr (ks + 5 < 24) {
        ha[ks + 5] = *(const bf16x8*)(himg + (ks + 5) * 1024 + (((ks + 5) & 1) ? so1 : so0));
        qb[ks + 5] = *(const bf16x8*)(qimg + (ks + 5) * 1024 + l * 16);
      }
      sc = MFMA(ha[ks], qb[ks], sc);
      SB();
    });
    float mx = sc[0];
#pragma unroll
    for (int i = 1; i < 16; ++i) mx = fmaxf(mx, sc[i]);
    mx = xmax(mx);
    float sum = 0.f;
#pragma unroll
    for (int i = 0; i < 16; ++i) { sc[i] = __builtin_amdgcn_exp2f(sc[i] - mx); sum += sc[i]; }
    const float inv = __builtin_amdgcn_rcpf(xsum(sum));
#pragma unroll
    for (int i = 0; i < 16; ++i) sc[i] *= inv;
    *(bf16x8*)(amix + l * 16) = pack8(sc, 0);
    *(bf16x8*)(amix + 1024 + l * 16) = pack8(sc, 1);
    lds_barrier();
  }
  SB();
  sfor<24, 34>(wissue);
  if (w != 0) lds_barrier();
  STAMP(4);
  {
    const int blk = (l >> 4) & 1, q = (l & 15) >> 2, p = l & 3, hh = p & 1;
    const int xr4 = 4 * (2 * blk + hh);
    const char* tb0 = himg + w * 2048 + blk * 1024 + (hh * 32 + ((4 * h + q) ^ xr4)) * 16 + 8 * (p >> 1);
    const char* tb1 = himg + w * 2048 + blk * 1024 + (hh * 32 + ((8 + 4 * h + q) ^ xr4)) * 16 + 8 * (p >> 1);
    f32x16 a = {};
    a = MFMA(tr_frag(tb0, tb1), *(const bf16x8*)(amix + l * 16), a);
    a = MFMA(tr_frag(tb0 + 256, tb1 + 256), *(const bf16x8*)(amix + 1024 + l * 16), a);
    *(bf16x8*)(qimg + (2 * w) * 1024 + l * 16) = pack8(a, 0);
    *(bf16x8*)(qimg + (2 * w + 1) * 1024 + l * 16) = pack8(a, 1);
  }
  SB();
  sfor<34, 36>(wissue);
  lds_barrier();
  STAMP(5);
  acc = f32x16{};
  {
    bf16x8 hf[24];
    hf[0] = *(const bf16x8*)(qimg + l * 16); hf[1] = *(const bf16x8*)(qimg + 1024 + l * 16);
    SB();
    sfor<0, 24>([&](auto I) {
      constexpr int ks = I.v;
      if constexpr (ks + 2 < 24) hf[ks + 2] = *(const bf16x8*)(qimg + (ks + 2) * 1024 + l * 16);
      if constexpr (ks < 12) wissue(IC<36 + ks>{});
      wwait<((ks < 12) ? 37 + ks : 48) - 1 - (24 + ks), false>(wf[24 + ks]);
      acc = MFMA(wf[24 + ks], hf[ks], acc);
      SB();
    });
  }
  STAMP(6);
  {
    f32x2 v[8];
#pragma unroll
    for (int i = 0; i < 8; ++i) v[i] = unpk(pk(unpk(xr[i >> 2][i & 3]) + f32x2{acc[2 * i], acc[2 * i + 1]}));
    partial_stats(v, st2, r, w, h);
    const int rr = n & 31;
    const size_t tile = ((size_t)t * NTILE + (n >> 5)) * TILEB + (size_t)(2 * w) * 1024;
    const int d0 = slot_off(0, h, rr), d1 = slot_off(1, h, rr);
    lds_barrier();
    float mean, rstd, vare;
    combine_stats(st2, r, mean, rstd, vare);
    const float nb = -mean * rstd;
    if (r < 28) {
#pragma unroll
      for (int i = 0; i < 2; ++i) {
        u32x4 p;
#pragma unroll
        for (int j = 0; j < 4; ++j) p[j] = pk(v[4 * i + j] * rstd + nb);
        gstore(H2 + tile + i * 1024 + (i ? d1 : d0), p);
      }
      if (w == 0 && h == 0) *(f32x2*)(X1 + ((size_t)t * (32 * NTILE) + n) * 8) = f32x2{mean, vare * rstd};
    }
  }
  STAMP(7);
  STOUT(X1 + (size_t)TF * NTILE * TILEB);
}

constexpr bool w2a(int f) { return f < 16 || f >= 52; }
DI void mfma_s(f32x16& acc, const bf16x8& a, const bf16x8& b) {
  asm volatile("v_mfma_f32_32x32x16_bf16 %0, %1, %2, %0" : "+v"(acc) : "v"(a), "v"(b));
}
DI void mfma_s_end(f32x16& acc, const bf16x8& a, const bf16x8& b) {
  asm volatile("v_mfma_f32_32x32x16_bf16 %0, %1, %2, %0\n\ts_nop 7\n\ts_nop 7" : "+v"(acc) : "v"(a), "v"(b));
}
template <bool NOPS> DI void mfma_s_move(f32x16& dst, const f32x16& acc, const bf16x8& a, const bf16x8& b) {
  if constexpr (NOPS) asm volatile("v_mfma_f32_32x32x16_bf16 %0, %2, %3, %1\n\ts_nop 7\n\ts_nop 7" : "=&v"(dst) : "v"(acc), "v"(a), "v"(b));
  else asm volatile("v_mfma_f32_32x32x16_bf16 %0, %2, %3, %1" : "=&v"(dst) : "v"(acc), "v"(a), "v"(b));
}
template <int HALF> DI void zero8(f32x16& a) {
#pragma unroll
  for (int i = 0; i < 8; ++i) asm volatile("v_accvgpr_write_b32 %0, 0" : "=a"(a[8 * HALF + i]));
}
DI void mfma_s0(f32x16& acc, const bf16x8& a, const bf16x8& b) {
  asm volatile("s_nop 1\n\tv_mfma_f32_32x32x16_bf16 %0, %1, %2, 0" : "=&v"(acc) : "v"(a), "v"(b));
}
DI float max3(float a, float b, float c) { float r; asm volatile("v_max3_f32 %0, %1, %2, %3" : "=v"(r) : "v"(a), "v"(b), "v"(c)); return r; }
constexpr int ev_slot(int e) { return e < 4 ? 2 * e + 1 : e < 20 ? 16 + 3 * ((e - 4) / 2) + ((e - 4) & 1) : 41 + 2 * (e - 20); }
constexpr int ev_at(int slot) { for (int e = 0; e < 24; ++e) if (ev_slot(e) == slot) return e; return -1; }
constexpr int ev_before(int slot) { int n = 0; for (int e = 0; e < 24; ++e) n += ev_slot(e) < slot; return n; }
constexpr bool xtra(int mode, int slot) { return mode == 1 && ((slot >= 8 && slot < 16) || slot == 18 || slot == 21 || slot == 24 || slot == 27); }
constexpr int xtra_before(int mode, int slot) { int n = 0; for (int i = 0; i < slot; ++i) n += xtra(mode, i); return n; }
constexpr int w2_before(int slot) { return xtra_before(1, slot) + (ev_before(slot) > 12 ? ev_before(slot) - 12 : 0); }
constexpr int younger(int mode, int f, int slot) {
  return ev_before(slot) + xtra_before(mode, slot) - 1 - (f - 12) - (f >= 12 ? xtra_before(mode, ev_slot(f - 12)) : 0);
}

__global__ __launch_bounds__(256, 1) void k2_spatial(const char* __restrict__ WA, const char* __restrict__ WB,
                                                     const char* __restrict__ H2, const char* __restrict__ X1,
                                                     unsigned short* __restrict__ out) {
  __shared__ __attribute__((aligned(16))) char lds[163840];
  const int t = blockIdx.x & 7, qt = blockIdx.x >> 3;
  if (t == 7) return;
  const int tid = threadIdx.x, w = __builtin_amdgcn_readfirstlane(tid >> 6), l = tid & 63, r = l & 31, h = l >> 5;
  asm volatile("" :: "s"(WB), "s"(X1), "s"(out));
  const unsigned voff = l * 16;
  const int so0 = slot_off(0, h, r), so1 = slot_off(1, h, r);
  unsigned vu[4];
  vu[0] = voff + (0 ^ w) * 2048 + 2048; vu[1] = voff + (1 ^ w) * 2048;
  vu[2] = voff + (2 ^ w) * 2048 + 2048; vu[3] = voff + (3 ^ w) * 2048;
  const char* keysm = H2 + (size_t)t * NTILE * TILEB - 2048;
  const int nt = (w < 3) ? 6 : 5;
  char* ring = lds + w * 40960;
  const unsigned ring0 = (unsigned)(size_t)ring;
  const char* wa4 = WA + (size_t)w * 72 * 1024 + 4096;
  const char* wb4 = WB + (size_t)w * 72 * 1024 + 4096;
  auto tile_of = [&](int jj) { const int i = 4 * jj + w + qt; return (i >= NTILE) ? i - NTILE : i; };
  auto dma_k = [&](auto K, const char* segsrc, unsigned m) {
    constexpr int k = K.v, cc = k >> 1;
    dma1<(k & 3) * 1024>(segsrc, m + (cc >= 2 ? 4096 : 0), vu[cc]);
  };
  u32x4 cb = {h ? 0u : 0xff610000u, 0u, 0u, 0u};
  auto sstart = [&](f32x16& sa, int tile) {
    const bool msk = (tile == NTILE - 1) && (r >= NP - 32 * (NTILE - 1));
    const u32x4 ca = {h ? 0u : (msk ? 0x3f803f80u : 0x3f80u), 0u, 0u, 0u};
    mfma_s0(sa, __builtin_bit_cast(bf16x8, ca), __builtin_bit_cast(bf16x8, cb));
  };

  STDECL; STAMP(0);
  bf16x8 qf[24];
  f32x16 scur, snext;
  f32x16 o[12];
  bf16x8 kc[2];
  {
    constexpr int WIN = 30;
    bf16x8 wf[72];
    {
      const char* src = keysm + 2048 + (size_t)qt * TILEB + w * 6144;
      sfor<0, 4>([&](auto I) { dma1<I.v * 1024>(src, ring0 + 4 * 8192, voff); });
      sfor<0, 2>([&](auto I) { dma1<I.v * 1024>(src + 4096, ring0 + 4 * 8192 + 4096, voff); });
    }
    const char* src0 = keysm + (size_t)tile_of(0) * TILEB;
    const char* src1 = keysm + (size_t)tile_of(1) * TILEB;
    auto stream = [&](auto I) {
      if constexpr (I.v < 72) wload<I.v, false>(wf[I.v], voff, wa4);
      else if constexpr (I.v < 96) dma_k(IC<(I.v - 72) & 7>{}, src0 + ((I.v - 72) >> 3) * 8192, ring0 + ((I.v - 72) >> 3) * 8192);
      else dma_k(IC<(I.v - 96) & 7>{}, src1 + ((I.v - 96) >> 3) * 8192, ring0 + (3 + ((I.v - 96) >> 3)) * 8192);
    };
    auto stream_z = [&](auto I) {
      stream(I);
      if constexpr (I.v < 24) zero8<I.v & 1>(o[I.v >> 1]);
    };
    sfor<0, 12>(stream_z);
    STAMP(1);
    wait_vm<12>();
    asm volatile("s_barrier" ::: "memory");
    sfor<12, WIN>(stream_z);
    STAMP(2);
    f32x16 acc[3] = {};
    bf16x8 hf[24];
    auto hread = [&](auto I) {
      hf[I.v] = *(const bf16x8*)(lds + (I.v / 6) * 40960 + 4 * 8192 + (I.v % 6) * 1024 + ((I.v & 1) ? so1 : so0));
    };
    sfor<0, 2>(hread);
    SB();
    sfor<0, 24>([&](auto I) {
      constexpr int ks = I.v;
      if constexpr (ks + 2 < 24) hread(IC<ks + 2>{});
      constexpr int i0 = (ks < 14) ? 3 * ks + WIN : 72 + 2 * (ks - 14), i1 = (ks < 14) ? i0 + 3 : i0 + 2;
      sfor<i0, i1>(stream);
      wwait<i1 - 3 * ks - 3, false>(wf[3 * ks + 2]); wtie<false>(wf[3 * ks]); wtie<false>(wf[3 * ks + 1]);
#pragma unroll
      for (int i = 0; i < 3; ++i) acc[i] = MFMA(wf[3 * ks + i], hf[ks], acc[i]);
      SB();
    });
    STAMP(3);
    sfor<0, 3>([&](auto I) {
#pragma unroll
      for (int s = 0; s < 2; ++s) *(bf16x8*)(ring + 3 * 8192 + (2 * I.v + s) * 1024 + l * 16) = pack8(acc[I.v], s);
      SB();
      stream(IC<92 + I.v>{});
    });
    lds_barrier();
    sfor<0, 4>([&](auto I) {
#pragma unroll
      for (int u = 3 * I.v; u < 3 * I.v + 3; ++u) {
        const int dt = (u & ~3) + ((u & 3) ^ w), ow = dt / 3;
        const char* p = lds + ow * 40960 + 3 * 8192 + (dt - 3 * ow) * 2048 + l * 16;
        qf[2 * u] = *(const bf16x8*)p; qf[2 * u + 1] = *(const bf16x8*)(p + 1024);
      }
      SB();
      if constexpr (I.v == 0) stream(IC<95>{});
    });
    lds_barrier();
    STAMP(4);
    bf16x8 kf[26];
    auto kread = [&](auto I) {
      constexpr int f = I.v;
      if constexpr ((f & 1) == 0) wait_vm<23 + ((f > 2) ? (f - 2) / 2 : 0) - (f + 1)>();
      kf[f] = *(const bf16x8*)(ring + f * 1024 + ((f & 1) ? so1 : so0));
    };
    sfor<0, 2>(kread);
    sstart(scur, tile_of(0));
    SB();
    sfor<0, 24>([&](auto I) {
      constexpr int f = I.v;
      kread(IC<f + 2>{});
      if constexpr (f & 1) stream(IC<96 + f / 2>{});
      if constexpr (f < 23) mfma_s(scur, kf[f], qf[f]); else mfma_s_end(scur, kf[f], qf[f]);
      SB();
    });
    kc[0] = kf[24]; kc[1] = kf[25];
  }

  const int blk = (l >> 4) & 1, q4 = (l & 15) >> 2, p4 = l & 3, hh = p4 & 1;
  const int xr4 = 4 * (2 * blk + hh);
  const int to0 = blk * 1024 + (hh * 32 + ((4 * h + q4) ^ xr4)) * 16 + 8 * (p4 >> 1);
  const int to1 = blk * 1024 + (hh * 32 + ((8 + 4 * h + q4) ^ xr4)) * 16 + 8 * (p4 >> 1);

  float mrun = 0.f;
  f32x2 lsum = {0.f, 0.f};
  bf16x8 w2[72];
  auto w2issue = [&](auto I) { if constexpr (I.v < 72) wload<I.v, w2a(I.v)>(w2[I.v], voff, wb4); };

  int sg0 = 0, sg1 = 8192, sg2 = 16384, sg3 = 24576, sg4 = 32768;
  const char* srcA = keysm + (size_t)tile_of(1) * TILEB;
  const char* srcB = keysm + (size_t)tile_of(2) * TILEB;
  auto period = [&](auto MODEC, int jj) {
    constexpr int MODE = MODEC.v;
    constexpr int PFD = 4;
    char* rb[3] = {ring + sg0, ring + sg1, ring + sg2};
    char* nb[4] = {ring + sg3, ring + sg4, ring + sg0, ring + sg1};
    auto stream = [&](auto S) {
      constexpr int e = ev_at(S.v);
      if constexpr (MODE == 2) {
        if constexpr (S.v < 28) w2issue(IC<24 + S.v>{});
      } else if constexpr (xtra(MODE, S.v)) w2issue(IC<w2_before(S.v)>{});
      else if constexpr (e >= 0) {
        if constexpr (e < 4) dma_k(IC<4 + e>{}, srcA + 8192, ring0 + sg4);
        else if constexpr (e < 12) dma_k(IC<e - 4>{}, srcA + 16384, ring0 + sg0);
        else if constexpr (MODE == 1) w2issue(IC<w2_before(S.v)>{});
        else if constexpr (e < 20) dma_k(IC<e - 12>{}, srcB, ring0 + sg1);
        else dma_k(IC<e - 20>{}, srcB + 8192, ring0 + sg2);
      }
    };
    bf16x8 kf[26], tf[24];
    kf[0] = kc[0]; kf[1] = kc[1];
    auto kread = [&](auto I, auto S) {
      constexpr int f = I.v;
      if constexpr (MODE == 0 || (MODE == 1 && f < 24)) {
        if constexpr ((f & 1) == 0) wait_vm<younger(MODE, f + 1, S.v)>();
        kf[f] = *(const bf16x8*)(nb[f >> 3] + (f & 7) * 1024 + ((f & 1) ? so1 : so0));
      }
    };
    auto pv_u = [](int p) { return p < 8 ? (p & 3) : (p >> 1); };
    auto pv_s = [](int p) { return p < 8 ? (p >> 2) : (p & 1); };
    auto tread = [&](auto I) {
      constexpr int u = pv_u(I.v);
      const char* b = rb[u >> 2] + (u & 3) * 2048 + pv_s(I.v) * 256;
      tf[I.v] = tr_frag(b + to0, b + to1);
    };
    float mx;
    if constexpr (MODE != 2) sstart(snext, tile_of(jj + 1));
    sfor<0, 3>([&](auto I) {
      kread(IC<I.v + 2>{}, I);
      if constexpr (MODE != 2) mfma_s(snext, kf[I.v], qf[I.v]);
      if constexpr (I.v == 1) mx = max3(max3(max3(max3(scur[0], scur[1], scur[2]), scur[3], scur[4]), scur[5], scur[6]), scur[7], scur[8]);
      if constexpr (I.v == 2) {
        mx = max3(max3(max3(max3(mx, scur[9], scur[10]), scur[11], scur[12]), scur[13], scur[14]), scur[15], scur[15]);
        const unsigned u = __builtin_bit_cast(unsigned, mx);
        const auto sw = __builtin_amdgcn_permlane32_swap(u, u, false, false);
        mx = max3(__builtin_bit_cast(float, (unsigned)sw[0]), __builtin_bit_cast(float, (unsigned)sw[1]), mx);
      }
      stream(I);
      SB();
    });
    if (jj == 0 || __any(mx > 64.f)) {
      const unsigned mb = pack2(mrun + ((jj == 0) ? mx : fmaxf(mx, 0.f)), 0.f) & 0xffffu;
      const float dl = bflo(mb) - mrun;
      mrun = bflo(mb);
      cb[0] = h ? 0u : (0xff610000u | (mb ^ 0x8000u));
#pragma unroll
      for (int i = 0; i < 16; ++i) scur[i] -= dl;
      if constexpr (MODE != 2) {
        asm volatile("s_nop 7\n\ts_nop 7\n\tv_sub_f32 %0, %0, %1" : "+v"(snext[0]) : "v"(dl));
#pragma unroll
        for (int i = 1; i < 15; ++i) asm volatile("v_sub_f32 %0, %0, %1" : "+v"(snext[i]) : "v"(dl));
        asm volatile("v_sub_f32 %0, %0, %1\n\ts_nop 1" : "+v"(snext[15]) : "v"(dl));
      }
      if (jj != 0) {
        const float f = __builtin_amdgcn_exp2f(-dl);
        lsum *= f;
#pragma unroll
        for (int c = 0; c < 12; ++c)
#pragma unroll
          for (int i = 0; i < 16; ++i) {
            float tmp;
            asm volatile("v_accvgpr_read_b32 %1, %0\n\tv_mul_f32 %1, %1, %2\n\tv_accvgpr_write_b32 %0, %1" : "+a"(o[c][i]), "=&v"(tmp) : "v"(f));
          }
        asm volatile("s_nop 4");
      }
    }
    SB();
    bf16x8 pf0, pf1;
    sfor<3, 8>([&](auto I) {
      constexpr int f = I.v;
      if constexpr (f + 2 < 8) kread(IC<f + 2>{}, I);
      if constexpr (f == 4) { tread(IC<1>{}); tread(IC<0>{}); }
      if constexpr (f == 6) { tread(IC<3>{}); tread(IC<2>{}); }
      if constexpr (MODE != 2) mfma_s(snext, kf[f], qf[f]);
      if constexpr (f < 7) {
        scur[2 * (f - 3)] = __builtin_amdgcn_exp2f(scur[2 * (f - 3)]); scur[2 * (f - 3) + 1] = __builtin_amdgcn_exp2f(scur[2 * (f - 3) + 1]);
      } else pf0 = pack8(scur, 0);
      stream(I);
      SB();
    });
    auto oput = [&](auto I) {
      constexpr int u = I.v, k = 3 * (u >> 2) + (u & 3) - 1;
      if constexpr ((u & 3) != 0) {
        char* dst = ((k < 4) ? nb[k >> 1] : rb[(k - 4) >> 1]) + (k & 1) * 4096 + l * 16;
#pragma unroll
        for (int g = 0; g < 4; ++g) *(f32x4*)(dst + g * 1024) = f32x4{o[u][4 * g], o[u][4 * g + 1], o[u][4 * g + 2], o[u][4 * g + 3]};
      }
    };
    const char* srcC = srcB;
    sfor<0, 24>([&](auto I) {
      constexpr int p = I.v;
      if constexpr ((p & 1) == 0 && p + PFD < 24) { tread(IC<p + PFD + 1>{}); tread(IC<p + PFD>{}); }
      if constexpr (p >= 22) kread(IC<p - 14>{}, IC<8 + p>{});
      o[pv_u(p)] = MFMA(tf[p], pv_s(p) ? pf1 : pf0, o[pv_u(p)]);
      if constexpr (p < 4) {
        scur[8 + 2 * p] = __builtin_amdgcn_exp2f(scur[8 + 2 * p]); scur[9 + 2 * p] = __builtin_amdgcn_exp2f(scur[9 + 2 * p]);
        if constexpr (p == 3) pf1 = pack8(scur, 1);
      } else if constexpr (p < 12) lsum += f32x2{scur[2 * (p - 4)], scur[2 * (p - 4) + 1]};
      if constexpr (MODE == 0 && p == 13) srcC = keysm + (size_t)tile_of(jj + 3) * TILEB;
      stream(IC<8 + p>{});
      if constexpr (MODE == 2 && p >= 7 && p < 10) oput(IC<p - 6>{});
      if constexpr (MODE == 2 && p >= 13 && (p & 1)) oput(IC<(p - 3) / 2>{});
      SB();
    });
    if constexpr (MODE == 2) oput(IC<11>{});
    else {
      sfor<8, 24>([&](auto I) {
        constexpr int f = I.v;
        kread(IC<f + 2>{}, IC<24 + f>{});
        if constexpr (f < 23) { mfma_s(snext, kf[f], qf[f]); stream(IC<24 + f>{}); }
        else { stream(IC<24 + f>{}); mfma_s_move<MODE == 1>(scur, snext, kf[f], qf[f]); }
        SB();
      });
      if constexpr (MODE == 0) { kc[0] = kf[24]; kc[1] = kf[25]; }
      const int t0 = sg0, t1 = sg1, t2 = sg2;
      sg0 = sg3; sg1 = sg4; sg2 = t0; sg3 = t1; sg4 = t2;
      srcA = srcB; srcB = srcC;
    }
  };
  {
    int jj = 0;
    do period(IC<0>{}, jj); while (++jj < nt - 2);
  }
  STAMP(5);
  period(IC<1>{}, nt - 2);
  STAMP(6);
  period(IC<2>{}, nt - 1);
  STAMP(7);

  const float ltot = xsum(lsum[0] + lsum[1]);
  auto oslot = [&](int sw, int k) {
    const int b = (sw < 3) ? 0 : 4;
    const int i = b + k + 6;
    return lds + sw * 40960 + ((i >= 10) ? i - 10 : i) * 4096;
  };
  if (h == 0) *(f32x2*)(oslot(w, 9) + r * 8) = f32x2{mrun, ltot};
  SB();
  sfor<52, 54>(w2issue);
  lds_barrier();
  float fs[4];
  {
    const f32x2 a = *(const f32x2*)(oslot(w, 9) + r * 8), b = *(const f32x2*)(oslot(w ^ 1, 9) + r * 8);
    const f32x2 c = *(const f32x2*)(oslot(w ^ 2, 9) + r * 8), d = *(const f32x2*)(oslot(w ^ 3, 9) + r * 8);
    const float m = fmaxf(fmaxf(a[0], b[0]), fmaxf(c[0], d[0]));
    fs[0] = __builtin_amdgcn_exp2f(a[0] - m); fs[1] = __builtin_amdgcn_exp2f(b[0] - m);
    fs[2] = __builtin_amdgcn_exp2f(c[0] - m); fs[3] = __builtin_amdgcn_exp2f(d[0] - m);
    const float inv = __builtin_amdgcn_rcpf(a[1] * fs[0] + b[1] * fs[1] + c[1] * fs[2] + d[1] * fs[3]);
    fs[0] *= inv; fs[1] *= inv; fs[2] *= inv; fs[3] *= inv;
  }
  f32x4 pa[4][3];
  u32x4 opk[2];
  auto mread = [&](auto SG, auto G) {
#pragma unroll
    for (int cc = 1; cc < 4; ++cc) pa[G.v][cc - 1] = *(const f32x4*)(oslot(w ^ cc, 3 * SG.v + cc - 1) + l * 16 + G.v * 1024);
  };
  auto mstep = [&](auto SG, auto G) {
    constexpr int sg = SG.v, g = G.v;
    f32x4 own;
#pragma unroll
    for (int e = 0; e < 4; ++e) {
      float tmp;
      asm volatile("v_accvgpr_read_b32 %0, %1" : "=v"(tmp) : "a"(o[4 * sg][4 * g + e]));
      own[e] = tmp;
    }
    own = own * fs[0] + pa[g][0] * fs[1] + pa[g][1] * fs[2] + pa[g][2] * fs[3];
    opk[g >> 1][2 * (g & 1)] = pack2(own[0], own[1]); opk[g >> 1][2 * (g & 1) + 1] = pack2(own[2], own[3]);
    if constexpr (g == 3) {
      char* dst = oslot(w ^ 1, 3 * sg) + l * 16;
      *(u32x4*)dst = opk[0]; *(u32x4*)(dst + 1024) = opk[1];
    }
  };
  sfor<0, 4>([&](auto G) { mread(IC<0>{}, G); });
  SB();
  sfor<54, 56>(w2issue);
  sfor<0, 4>([&](auto G) { mstep(IC<0>{}, G); });
  lds_barrier();
  STAMP(8);
  f32x16 acc[3] = {};
  u32x4 xv[6];
  f32x2 ms;
  {
    const char* x1 = H2 + ((size_t)t * NTILE + qt) * TILEB + (size_t)(6 * w) * 1024 + 2048;
    const char* xa = x1 + so0;
    const char* xb = x1 + so1;
    bf16x8 of[24];
    auto oread = [&](auto I) {
      constexpr int ks = I.v;
      of[ks] = *(const bf16x8*)(oslot(((ks >> 1) & 3) ^ 1, 3 * (ks >> 3)) + (ks & 1) * 1024 + l * 16);
    };
    sfor<0, 3>([&](auto SG) {
      constexpr int sg = SG.v;
      sfor<8 * sg, 8 * sg + 2>(oread);
      SB();
      sfor<0, 8>([&](auto K) {
        constexpr int k = K.v, ks = 8 * sg + k;
        if constexpr (k + 2 < 8) oread(IC<ks + 2>{});
        if constexpr (sg < 2 && k < 4) mread(IC<sg + 1>{}, K);
        if constexpr (ks < 8) sfor<56 + 2 * ks, 58 + 2 * ks>(w2issue);
        else if constexpr (ks < 14) gload_p<(ks - 8) * 1024 - 2048>(xv[ks - 8], ((ks - 8) & 1) ? xb : xa);
        else if constexpr (ks == 14)
          asm volatile("global_load_dwordx2 %0, %1, off" : "=v"(ms) : "v"(X1 + ((size_t)t * (32 * NTILE) + 32 * qt + r) * 8));
        constexpr int issued = (ks < 8) ? 58 + 2 * ks : ((ks < 15) ? 72 + (ks - 7) : 79);
        wwait<issued - 3 * ks - 3, w2a(3 * ks + 2)>(w2[3 * ks + 2]); wtie<w2a(3 * ks)>(w2[3 * ks]); wtie<w2a(3 * ks + 1)>(w2[3 * ks + 1]);
#pragma unroll
        for (int i = 0; i < 3; ++i) acc[i] = MFMA(w2[3 * ks + i], of[ks], acc[i]);
        if constexpr (sg < 2 && k >= 2 && k < 6) mstep(IC<sg + 1>{}, IC<k - 2>{});
        SB();
      });
      if constexpr (sg < 2) lds_barrier();
    });
  }
  STAMP(9);
  wait6<0>(xv);
  asm volatile("" : "+v"(ms));
  {
    char* ob0 = oslot(w ^ 2, 1);
    char* ob1 = oslot(w ^ 3, 2) - 16 * 208;
#pragma unroll
    for (int i = 0; i < 6; ++i) {
      u32x4 p;
#pragma unroll
      for (int j = 0; j < 4; ++j)
        p[j] = pk(unpk(xv[i][j]) * ms[1] + (ms[0] + f32x2{acc[i >> 1][8 * (i & 1) + 2 * j], acc[i >> 1][8 * (i & 1) + 2 * j + 1]}));
      *(u32x4*)(((r < 16) ? ob0 : ob1) + r * 208 + i * 32 + h * 16) = p;
    }
    unsigned short* obase = out + ((size_t)t * NP + 32 * qt) * CH + 96 * w;
#pragma unroll
    for (int i = 0; i < 6; ++i) {
      const int id = i * 64 + l, row = id / 12, ch = id - 12 * row;
      const u32x4 p = *(const u32x4*)(((i < 3) ? ob0 : ob1) + row * 208 + ch * 16);
      if (32 * qt + row < NP) *(u32x4*)(obase + (size_t)row * CH + 8 * ch) = p;
    }
  }
  STAMP(10);
  STOUT((char*)X1 + (size_t)TF * NTILE * TILEB + 1048576);
}

extern "C" void kernel_launch(void* const* d_in, const int* in_sizes, int n_in,
                              void* d_out, int out_size, void* d_ws, size_t ws_size,
                              hipStream_t stream) {
  const unsigned short* x = (const unsigned short*)d_in[0];
  const unsigned short* wqkv_t = (const unsigned short*)d_in[1];
  const unsigned short* wo_t = (const unsigned short*)d_in[2];
  const unsigned short* wqkv_s = (const unsigned short*)d_in[3];
  const unsigned short* wo_s = (const unsigned short*)d_in[4];
  char* ws = (char*)d_ws;
  k0_fold<<<144, 256, 0, stream>>>(wqkv_t, wo_t, wqkv_s, wo_s, ws);
  k1_temporal<<<184, 768, 0, stream>>>(x, ws + OFF_WA_T, ws + OFF_WB_T, ws + OFF_H2, ws + OFF_X1);
  k2_spatial<<<184, 256, 0, stream>>>(ws + OFF_WA_S, ws + OFF_WB_S, ws + OFF_H2, ws + OFF_X1, (unsigned short*)d_out);
}
```

```cpp
#include <hip/hip_runtime.h>
#include <hip/hip_bf16.h>

using bf16x8 = __attribute__((ext_vector_type(8))) __bf16;
using bf16x2 = __attribute__((ext_vector_type(2))) __bf16;
using s16x4  = __attribute__((ext_vector_type(4))) short;
using s16x8  = __attribute__((ext_vector_type(8))) short;
using f32x16 = __attribute__((ext_vector_type(16))) float;
using f32x4  = __attribute__((ext_vector_type(4))) float;
using f32x2  = __attribute__((ext_vector_type(2))) float;
using u32x4  = __attribute__((ext_vector_type(4))) unsigned;
typedef __attribute__((address_space(3))) s16x4 lds_s16x4;

#define DI __device__ __forceinline__
#define MFMA(a, b, c) __builtin_amdgcn_mfma_f32_32x32x16_bf16((a), (b), (c), 0, 0, 0)

constexpr int TF = 7, NP = 729, CH = 384;
constexpr int NTILE = 23;
constexpr int WBYTES = 12 * 24 * 1024;
constexpr int TILEB = 24 * 1024;
constexpr size_t OFF_WA_T = 0, OFF_WB_T = WBYTES, OFF_WA_S = 2 * WBYTES, OFF_WB_S = 3 * WBYTES;
constexpr size_t OFF_H2 = 4 * (size_t)WBYTES;
constexpr size_t OFF_X1 = OFF_H2 + (size_t)TF * NTILE * TILEB;

DI int perm23(int r) { return (r & 0x13) | ((r & 4) << 1) | ((r & 8) >> 1); }
DI int slot_off(int ksodd, int h, int r) { return (h * 32 + (r ^ (4 * (2 * ksodd + h)))) * 16; }

DI unsigned pack2(float a, float b) {
  f32x2 p = {a, b};
  return __builtin_bit_cast(unsigned, __builtin_convertvector(p, bf16x2));
}
DI bf16x8 pack8(const f32x16& x, int s) {
  u32x4 p;
  p[0] = pack2(x[8 * s + 0], x[8 * s + 1]); p[1] = pack2(x[8 * s + 2], x[8 * s + 3]);
  p[2] = pack2(x[8 * s + 4], x[8 * s + 5]); p[3] = pack2(x[8 * s + 6], x[8 * s + 7]);
  return __builtin_bit_cast(bf16x8, p);
}
DI float bflo(unsigned u) { return __builtin_bit_cast(float, u << 16); }
DI float bfhi(unsigned u) { return __builtin_bit_cast(float, u & 0xffff0000u); }
DI f32x2 unpk(unsigned u) { return f32x2{bflo(u), bfhi(u)}; }
DI unsigned pk(f32x2 p) { return __builtin_bit_cast(unsigned, __builtin_convertvector(p, bf16x2)); }
DI float xsum(float v) {
  const unsigned u = __builtin_bit_cast(unsigned, v);
  const auto r = __builtin_amdgcn_permlane32_swap(u, u, false, false);
  return __builtin_bit_cast(float, (unsigned)r[0]) + __builtin_bit_cast(float, (unsigned)r[1]);
}
DI float xmax(float v) {
  const unsigned u = __builtin_bit_cast(unsigned, v);
  const auto r = __builtin_amdgcn_permlane32_swap(u, u, false, false);
  return fmaxf(__builtin_bit_cast(float, (unsigned)r[0]), __builtin_bit_cast(float, (unsigned)r[1]));
}
DI void lds_barrier() { asm volatile("s_waitcnt lgkmcnt(0)\n\ts_barrier" ::: "memory"); }
#define SB() __builtin_amdgcn_sched_barrier(0)
#define STDECL
#define STAMP(i)
#define STACC(i, prev)
#define STOUT(buf)
template <int I> struct IC { static constexpr int v = I; };
template <int B, int E, class F> DI void sfor(F&& f) { if constexpr (B < E) { f(IC<B>{}); sfor<B + 1, E>(f); } }
template <int OFF> DI void gload_a(bf16x8& dst, unsigned voff, const char* sbase) {
  asm volatile("global_load_dwordx4 %0, %1, %2 offset:%3" : "=a"(dst) : "v"(voff), "s"(sbase), "i"(OFF));
}
template <int OFF> DI void gload_v(bf16x8& dst, unsigned voff, const char* sbase) {
  asm volatile("global_load_dwordx4 %0, %1, %2 offset:%3" : "=v"(dst) : "v"(voff), "s"(sbase), "i"(OFF));
}
template <int OFF> DI void gload_p(u32x4& dst, const void* p) {
  asm volatile("global_load_dwordx4 %0, %1, off offset:%2" : "=v"(dst) : "v"(p), "i"(OFF));
}
template <int IDX, bool AG> DI void wload(bf16x8& dst, unsigned voff, const char* base4k) {
  if constexpr (AG) gload_a<(IDX % 8) * 1024 - 4096>(dst, voff, base4k + (IDX / 8) * 8192);
  else gload_v<(IDX % 8) * 1024 - 4096>(dst, voff, base4k + (IDX / 8) * 8192);
}
template <int N, bool AG> DI void wwait(bf16x8& a) {
  constexpr int M = (N > 63) ? 63 : N;
  if constexpr (AG) asm volatile("s_waitcnt vmcnt(%1)" : "+a"(a) : "i"(M));
  else asm volatile("s_waitcnt vmcnt(%1)" : "+v"(a) : "i"(M));
}
template <bool AG> DI void wtie(bf16x8& a) {
  if constexpr (AG) asm volatile("" : "+a"(a)); else asm volatile("" : "+v"(a));
}
template <int N> DI void wait6(u32x4 (&x)[6]) {
  asm volatile("s_waitcnt vmcnt(%6)" : "+v"(x[0]), "+v"(x[1]), "+v"(x[2]), "+v"(x[3]), "+v"(x[4]), "+v"(x[5]) : "i"(N));
}

DI bf16x8 tr_frag(const char* p0, const char* p1) {
  s16x4 lo = __builtin_amdgcn_ds_read_tr16_b64_v4i16((lds_s16x4*)p0);
  s16x4 hi = __builtin_amdgcn_ds_read_tr16_b64_v4i16((lds_s16x4*)p1);
  s16x8 v = __builtin_shufflevector(lo, hi, 0, 1, 2, 3, 4, 5, 6, 7);
  return __builtin_bit_cast(bf16x8, v);
}

template <int IMM> DI void dma1(const char* srcm, unsigned m0v, unsigned voff) {
  asm volatile("s_mov_b32 m0, %1\n\ts_nop 0\n\tglobal_load_lds_dwordx4 %2, %0 offset:%3"
               :: "s"(srcm), "s"(m0v), "v"(voff), "i"(IMM) : "memory");
}
template <int N> DI void wait_vm() { asm volatile("s_waitcnt vmcnt(%0)" :: "i"(N) : "memory"); }

DI void gstore(void* p, u32x4 v) {
  asm volatile("global_store_dwordx4 %0, %1, off sc1\n\ts_nop 1" :: "v"(p), "v"(v) : "memory");
}
DI void dma1p(const void* p, unsigned m0v) {
  asm volatile("s_mov_b32 m0, %1\n\ts_nop 0\n\tglobal_load_lds_dwordx4 %0, off" :: "v"(p), "s"(m0v) : "memory");
}

__global__ __launch_bounds__(256) void k0_fold(const unsigned short* __restrict__ wqkv_t, const unsigned short* __restrict__ wo_t,
                                               const unsigned short* __restrict__ wqkv_s, const unsigned short* __restrict__ wo_s,
                                               char* __restrict__ ws) {
  __shared__ __attribute__((aligned(16))) char lds[2 * 49152];
  char* la = lds;
  char* lb = lds + 49152;
  const int b = blockIdx.x, prod = b / 36, rem = b % 36, nb = rem / 6, cb = rem % 6;
  const int tid = threadIdx.x, w = __builtin_amdgcn_readfirstlane(tid >> 6), l = tid & 63, r = l & 31, h = l >> 5;
  const unsigned short* wqkv = (prod < 2) ? wqkv_t : wqkv_s;
  const unsigned short* wo = (prod < 2) ? wo_t : wo_s;
  const bool isB = prod & 1;
  STDECL; STAMP(0);
  {
    const unsigned short* asrc = wqkv + (size_t)(64 * cb + 16 * w) * 1152 + (isB ? 768 : 0);
    const unsigned short* bsrc = wqkv + (size_t)(64 * nb + 16 * w) * 1152 + 384;
    const unsigned short* osrc = wo + (size_t)(96 * w + (l >> 3)) * 384 + 64 * nb + 8 * (l & 7);
    const unsigned la0 = (unsigned)(size_t)la + w * 12288, lb0 = (unsigned)(size_t)lb + w * 12288;
#pragma unroll
    for (int j = 0; j < 3; ++j) {
      const int id = 64 * j + l, row = id / 48, pos = id - 48 * row;
#pragma unroll
      for (int g = 0; g < 4; ++g) {
        const int rw = 4 * g + row;
        const int off = rw * 1152 + 8 * ((pos & ~15) | ((pos ^ rw) & 15));
        dma1p(asrc + off, la0 + (3 * g + j) * 1024);
        if (!isB) dma1p(bsrc + off, lb0 + (3 * g + j) * 1024);
        else dma1p(osrc + (size_t)(8 * (3 * g + j)) * 384, lb0 + (3 * g + j) * 1024);
      }
    }
  }
  STAMP(1);
  const int ct = w & 1, nt = w >> 1;
  const int ra = 32 * ct + perm23(r), rb = 32 * nt + perm23(r);
  int aa[8], ab[8];
#pragma unroll
  for (int k = 0; k < 8; ++k) { aa[k] = ra * 768 + (((2 * k + h) ^ ra) & 15) * 16; ab[k] = rb * 768 + (((2 * k + h) ^ rb) & 15) * 16; }
  const int blk = (l >> 4) & 1, q = (l & 15) >> 2, p = l & 3;
  const char* tb = lb + (8 * h + q) * 128 + (32 * nt + 16 * blk + 8 * (p & 1) + 4 * (p >> 1)) * 2;
  asm volatile("s_waitcnt vmcnt(0)\n\ts_barrier" ::: "memory");
  STAMP(3);
  f32x16 acc = {};
  bf16x8 af[24], bf[24];
  auto rd = [&](auto I, auto ISB) {
    constexpr int ks = I.v;
    af[ks] = *(const bf16x8*)(la + aa[ks & 7] + (ks >> 3) * 256);
    if constexpr (ISB.v) bf[ks] = tr_frag(tb + ks * 2048, tb + ks * 2048 + 512);
    else bf[ks] = *(const bf16x8*)(lb + ab[ks & 7] + (ks >> 3) * 256);
  };
  auto gemm = [&](auto ISB) {
    sfor<0, 4>([&](auto I) { rd(I, ISB); });
    SB();
    sfor<0, 24>([&](auto I) {
      if constexpr (I.v + 4 < 24) rd(IC<I.v + 4>{}, ISB);
      acc = MFMA(af[I.v], bf[I.v], acc);
      SB();
    });
  };
  if (!isB) {
    gemm(IC<0>{});
    const float sc = 0.05103103630798288f * 1.4426950408889634f;
#pragma unroll
    for (int i = 0; i < 16; ++i) acc[i] *= sc;
  } else gemm(IC<1>{});
  STAMP(4);
  char* dst = ws + (size_t)prod * WBYTES;
  const int ntile = 2 * nb + nt, ctile = 2 * cb + ct;
#pragma unroll
  for (int s = 0; s < 2; ++s) {
    const int ks = 2 * ctile + s;
    const int idx = (prod < 2) ? ntile * 24 + ks : ((ntile / 3) * 24 + ks) * 3 + ntile % 3;
    gstore(dst + (size_t)idx * 1024 + l * 16, __builtin_bit_cast(u32x4, pack8(acc, s)));
  }
  STAMP(5);
  STOUT(ws + OFF_X1 + (size_t)TF * NTILE * TILEB + 2097152);
}

DI void partial_stats(const f32x2 (&v)[8], char* st, int r, int w, int h) {
  f32x2 s = v[0];
#pragma unroll
  for (int i = 1; i < 8; ++i) s += v[i];
  const float mean_w = xsum(s[0] + s[1]) * (1.f / 32.f);
  f32x2 q = {0.f, 0.f};
#pragma unroll
  for (int i = 0; i < 8; ++i) { const f32x2 d = v[i] - mean_w; q += d * d; }
  const float m2_w = xsum(q[0] + q[1]);
  *(float*)(st + r * 112 + h * 48 + w * 4) = h ? m2_w : mean_w;
}
DI void combine_stats(const char* st, int r, float& mean, float& rstd, float& vare) {
  f32x4 a[6];
#pragma unroll
  for (int i = 0; i < 6; ++i) a[i] = *(const f32x4*)(st + r * 112 + 16 * i);
  const f32x4 ms = a[0] + a[1] + a[2], qs = a[3] + a[4] + a[5];
  const float m = (ms[0] + ms[1] + ms[2] + ms[3]) * (1.f / 12.f);
  const f32x4 d0 = a[0] - m, d1 = a[1] - m, d2 = a[2] - m;
  const f32x4 e = qs + 32.f * (d0 * d0 + d1 * d1 + d2 * d2);
  mean = m;
  vare = (e[0] + e[1] + e[2] + e[3]) * (1.f / 384.f) + 1e-5f;
  rstd = __builtin_amdgcn_rsqf(vare);
}
template <int N> DI void wait2(u32x4 (&x)[2]) { asm volatile("s_waitcnt vmcnt(%2)" : "+v"(x[0]), "+v"(x[1]) : "i"(N)); }

__global__ __launch_bounds__(768, 3) void k1_temporal(const unsigned short* __restrict__ x, const char* __restrict__ WA,
                                                      const char* __restrict__ WB, char* __restrict__ H2, char* __restrict__ X1) {
  __shared__ __attribute__((aligned(16))) char lds[2 * TILEB + 2048 + 2 * 3584];
  char* himg = lds;
  char* qimg = lds + TILEB;
  char* amix = lds + 2 * TILEB;
  char* st1 = lds + 2 * TILEB + 2048;
  char* st2 = st1 + 3584;
  const int tid = threadIdx.x, w = __builtin_amdgcn_readfirstlane(tid >> 6), l = tid & 63, r = l & 31, h = l >> 5;
  const int pos = r & 3, t = r >> 2, n = 4 * blockIdx.x + pos;
  const bool valid = (r < 28) && (n < NP);
  STDECL; STAMP(0);
  asm volatile("" :: "s"(H2), "s"(X1), "s"(WB));
  bf16x8 wf[48];
  const unsigned voff = l * 16;
  const char* wa4 = WA + (size_t)w * 24 * 1024 + 4096;
  const char* wb4 = WB + (size_t)w * 24 * 1024 + 4096;
  auto wissue = [&](auto I) {
    constexpr int f = I.v;
    if constexpr (f < 24) wload<f, false>(wf[f], voff, wa4);
    else if constexpr (f < 48) wload<f - 24, false>(wf[f], voff, wb4);
  };
  const int so0 = slot_off(0, h, r), so1 = slot_off(1, h, r);
  u32x4 xr[2];
  {
#pragma unroll
    for (int i = 0; i < 2; ++i) {
      const int id = (2 * w + i) * 64 + l, row0 = id / 48, ps = id - 48 * row0;
      const int ch = (ps & ~15) | ((ps ^ row0) & 15);
      const int row = (row0 < 28) ? row0 : 27, nn = min(4 * (int)blockIdx.x + (row & 3), NP - 1);
      dma1p(x + ((size_t)(row >> 2) * NP + nn) * CH + 8 * ch, (unsigned)(size_t)qimg + (2 * w + i) * 1024);
    }
    asm volatile("s_barrier" ::: "memory");
    sfor<0, 4>(wissue);
    wait_vm<4>();
    asm volatile("s_barrier" ::: "memory");
    STAMP(1);
    const char* xp = qimg + r * 768 + (w >> 2) * 256;
    xr[0] = *(const u32x4*)(xp + (((4 * w + h) ^ r) & 15) * 16); xr[1] = *(const u32x4*)(xp + (((4 * w + 2 + h) ^ r) & 15) * 16);
#pragma unroll
    for (int i = 0; i < 2; ++i) if (!valid) xr[i] = u32x4{0, 0, 0, 0};
    f32x2 v[8];
#pragma unroll
    for (int i = 0; i < 8; ++i) v[i] = unpk(xr[i >> 2][i & 3]);
    partial_stats(v, st1, r, w, h);
    SB();
    sfor<4, 9>(wissue);
    lds_barrier();
    float mean, rstd, vare;
    combine_stats(st1, r, mean, rstd, vare);
    const float nb = -mean * rstd;
#pragma unroll
    for (int i = 0; i < 2; ++i) {
      u32x4 p;
#pragma unroll
      for (int j = 0; j < 4; ++j) p[j] = pk(v[4 * i + j] * rstd + nb);
      *(u32x4*)(himg + (2 * w + i) * 1024 + (i ? so1 : so0)) = p;
    }
    SB();
    sfor<9, 14>(wissue);
    lds_barrier();
  }
  STAMP(2);
  f32x16 acc = {};
  {
    bf16x8 hf[24];
    hf[0] = *(const bf16x8*)(himg + so0); hf[1] = *(const bf16x8*)(himg + 1024 + so1);
    SB();
    sfor<0, 24>([&](auto I) {
      constexpr int ks = I.v;
      if constexpr (ks + 2 < 24) hf[ks + 2] = *(const bf16x8*)(himg + (ks + 2) * 1024 + ((ks & 1) ? so1 : so0));
      constexpr int iss = (ks < 10) ? 15 + ks : 24;
      if constexpr (ks < 10) wissue(IC<iss - 1>{});
      wwait<iss - 1 - ks, false>(wf[ks]);
      acc = MFMA(wf[ks], hf[ks], acc);
      SB();
    });
  }
  STAMP(3);
  *(bf16x8*)(qimg + (2 * w) * 1024 + l * 16) = pack8(acc, 0);
  *(bf16x8*)(qimg + (2 * w + 1) * 1024 + l * 16) = pack8(acc, 1);
  lds_barrier();
  if (w == 0) {
    f32x16 sc;
#pragma unroll
    for (int i = 0; i < 16; ++i) {
      const int row = (i & 3) + 8 * (i >> 2) + 4 * h;
      sc[i] = ((i & 3) == pos && row < 28) ? 0.f : -3.0e38f;
    }
    bf16x8 ha[24], qb[24];
    sfor<0, 5>([&](auto I) {
      ha[I.v] = *(const bf16x8*)(himg + I.v * 1024 + ((I.v & 1) ? so1 : so0));
      qb[I.v] = *(const bf16x8*)(qimg + I.v * 1024 + l * 16);
    });
    SB();
    sfor<0, 24>([&](auto I) {
      constexpr int ks = I.v;
      if constexpr (ks + 5 < 24) {
        ha[ks + 5] = *(const bf16x8*)(himg + (ks + 5) * 1024 + (((ks + 5) & 1) ? so1 : so0));
        qb[ks + 5] = *(const bf16x8*)(qimg + (ks + 5) * 1024 + l * 16);
      }
      sc = MFMA(ha[ks], qb[ks], sc);
      SB();
    });
    float mx = sc[0];
#pragma unroll
    for (int i = 1; i < 16; ++i) mx = fmaxf(mx, sc[i]);
    mx = xmax(mx);
    float sum = 0.f;
#pragma unroll
    for (int i = 0; i < 16; ++i) { sc[i] = __builtin_amdgcn_exp2f(sc[i] - mx); sum += sc[i]; }
    const float inv = __builtin_amdgcn_rcpf(xsum(sum));
#pragma unroll
    for (int i = 0; i < 16; ++i) sc[i] *= inv;
    *(bf16x8*)(amix + l * 16) = pack8(sc, 0);
    *(bf16x8*)(amix + 1024 + l * 16) = pack8(sc, 1);
    lds_barrier();
  }
  SB();
  sfor<24, 34>(wissue);
  if (w != 0) lds_barrier();
  STAMP(4);
  {
    const int blk = (l >> 4) & 1, q = (l & 15) >> 2, p = l & 3, hh = p & 1;
    const int xr4 = 4 * (2 * blk + hh);
    const char* tb0 = himg + w * 2048 + blk * 1024 + (hh * 32 + ((4 * h + q) ^ xr4)) * 16 + 8 * (p >> 1);
    const char* tb1 = himg + w * 2048 + blk * 1024 + (hh * 32 + ((8 + 4 * h + q) ^ xr4)) * 16 + 8 * (p >> 1);
    f32x16 a = {};
    a = MFMA(tr_frag(tb0, tb1), *(const bf16x8*)(amix + l * 16), a);
    a = MFMA(tr_frag(tb0 + 256, tb1 + 256), *(const bf16x8*)(amix + 1024 + l * 16), a);
    *(bf16x8*)(qimg + (2 * w) * 1024 + l * 16) = pack8(a, 0);
    *(bf16x8*)(qimg + (2 * w + 1) * 1024 + l * 16) = pack8(a, 1);
  }
  SB();
  sfor<34, 36>(wissue);
  lds_barrier();
  STAMP(5);
  acc = f32x16{};
  {
    bf16x8 hf[24];
    hf[0] = *(const bf16x8*)(qimg + l * 16); hf[1] = *(const bf16x8*)(qimg + 1024 + l * 16);
    SB();
    sfor<0, 24>([&](auto I) {
      constexpr int ks = I.v;
      if constexpr (ks + 2 < 24) hf[ks + 2] = *(const bf16x8*)(qimg + (ks + 2) * 1024 + l * 16);
      if constexpr (ks < 12) wissue(IC<36 + ks>{});
      wwait<((ks < 12) ? 37 + ks : 48) - 1 - (24 + ks), false>(wf[24 + ks]);
      acc = MFMA(wf[24 + ks], hf[ks], acc);
      SB();
    });
  }
  STAMP(6);
  {
    f32x2 v[8];
#pragma unroll
    for (int i = 0; i < 8; ++i) v[i] = unpk(pk(unpk(xr[i >> 2][i & 3]) + f32x2{acc[2 * i], acc[2 * i + 1]}));
    partial_stats(v, st2, r, w, h);
    const int rr = n & 31;
    const size_t tile = ((size_t)t * NTILE + (n >> 5)) * TILEB + (size_t)(2 * w) * 1024;
    const int d0 = slot_off(0, h, rr), d1 = slot_off(1, h, rr);
    lds_barrier();
    float mean, rstd, vare;
    combine_stats(st2, r, mean, rstd, vare);
    const float nb = -mean * rstd;
    if (r < 28) {
#pragma unroll
      for (int i = 0; i < 2; ++i) {
        u32x4 p;
#pragma unroll
        for (int j = 0; j < 4; ++j) p[j] = pk(v[4 * i + j] * rstd + nb);
        gstore(H2 + tile + i * 1024 + (i ? d1 : d0), p);
      }
      if (w == 0 && h == 0) *(f32x2*)(X1 + ((size_t)t * (32 * NTILE) + n) * 8) = f32x2{mean, vare * rstd};
    }
  }
  STAMP(7);
  STOUT(X1 + (size_t)TF * NTILE * TILEB);
}

constexpr bool w2a(int f) { return f < 16 || f >= 52; }
DI void mfma_s(f32x16& acc, const bf16x8& a, const bf16x8& b) {
  asm volatile("v_mfma_f32_32x32x16_bf16 %0, %1, %2, %0" : "+v"(acc) : "v"(a), "v"(b));
}
DI void mfma_s_end(f32x16& acc, const bf16x8& a, const bf16x8& b) {
  asm volatile("v_mfma_f32_32x32x16_bf16 %0, %1, %2, %0\n\ts_nop 7\n\ts_nop 7" : "+v"(acc) : "v"(a), "v"(b));
}
template <bool NOPS> DI void mfma_s_move(f32x16& dst, const f32x16& acc, const bf16x8& a, const bf16x8& b) {
  if constexpr (NOPS) asm volatile("v_mfma_f32_32x32x16_bf16 %0, %2, %3, %1\n\ts_nop 7\n\ts_nop 7" : "=&v"(dst) : "v"(acc), "v"(a), "v"(b));
  else asm volatile("v_mfma_f32_32x32x16_bf16 %0, %2, %3, %1" : "=&v"(dst) : "v"(acc), "v"(a), "v"(b));
}
template <int HALF> DI void zero8(f32x16& a) {
#pragma unroll
  for (int i = 0; i < 8; ++i) asm volatile("v_accvgpr_write_b32 %0, 0" : "=a"(a[8 * HALF + i]));
}
DI void mfma_s0(f32x16& acc, const bf16x8& a, const bf16x8& b) {
  asm volatile("s_nop 1\n\tv_mfma_f32_32x32x16_bf16 %0, %1, %2, 0" : "=&v"(acc) : "v"(a), "v"(b));
}
DI float max3(float a, float b, float c) { float r; asm volatile("v_max3_f32 %0, %1, %2, %3" : "=v"(r) : "v"(a), "v"(b), "v"(c)); return r; }
constexpr int ev_slot(int e) { return e < 4 ? 2 * e + 1 : e < 20 ? 16 + 3 * ((e - 4) / 2) + ((e - 4) & 1) : 41 + 2 * (e - 20); }
constexpr int ev_at(int slot) { for (int e = 0; e < 24; ++e) if (ev_slot(e) == slot) return e; return -1; }
constexpr int ev_before(int slot) { int n = 0; for (int e = 0; e < 24; ++e) n += ev_slot(e) < slot; return n; }

__global__ __launch_bounds__(256, 1) void k2_spatial(const char* __restrict__ WA, const char* __restrict__ WB,
                                                     const char* __restrict__ H2, const char* __restrict__ X1,
                                                     unsigned short* __restrict__ out) {
  __shared__ __attribute__((aligned(16))) char lds[163840];
  const int t = blockIdx.x & 7, qt = blockIdx.x >> 3;
  if (t == 7) return;
  const int tid = threadIdx.x, w = __builtin_amdgcn_readfirstlane(tid >> 6), l = tid & 63, r = l & 31, h = l >> 5;
  asm volatile("" :: "s"(WB), "s"(X1), "s"(out));
  const unsigned voff = l * 16;
  const int so0 = slot_off(0, h, r), so1 = slot_off(1, h, r);
  unsigned vu[4];
  vu[0] = voff + (0 ^ w) * 2048 + 2048; vu[1] = voff + (1 ^ w) * 2048;
  vu[2] = voff + (2 ^ w) * 2048 + 2048; vu[3] = voff + (3 ^ w) * 2048;
  const char* keysm = H2 + (size_t)t * NTILE * TILEB - 2048;
  const int nt = (w < 3) ? 6 : 5;
  char* ring = lds + w * 40960;
  const unsigned ring0 = (unsigned)(size_t)ring;
  const char* wa4 = WA + (size_t)w * 72 * 1024 + 4096;
  const char* wb4 = WB + (size_t)w * 72 * 1024 + 4096;
  auto tile_of = [&](int jj) { const int i = 4 * jj + w + qt; return (i >= NTILE) ? i - NTILE : i; };
  auto dma_k = [&](auto K, const char* segsrc, unsigned m) {
    constexpr int k = K.v, cc = k >> 1;
    dma1<(k & 3) * 1024>(segsrc, m + (cc >= 2 ? 4096 : 0), vu[cc]);
  };
  u32x4 cb = {h ? 0u : 0xff610000u, 0u, 0u, 0u};
  auto sstart = [&](f32x16& sa, int tile) {
    const bool msk = (tile == NTILE - 1) && (r >= NP - 32 * (NTILE - 1));
    const u32x4 ca = {h ? 0u : (msk ? 0x3f803f80u : 0x3f80u), 0u, 0u, 0u};
    mfma_s0(sa, __builtin_bit_cast(bf16x8, ca), __builtin_bit_cast(bf16x8, cb));
  };

  STDECL; STAMP(0);
  bf16x8 qf[24];
  f32x16 scur, snext;
  f32x16 o[12];
  bf16x8 kc[2];
  {
    constexpr int WIN = 30;
    bf16x8 wf[72];
    {
      const char* src = keysm + 2048 + (size_t)qt * TILEB + w * 6144;
      sfor<0, 4>([&](auto I) { dma1<I.v * 1024>(src, ring0 + 4 * 8192, voff); });
      sfor<0, 2>([&](auto I) { dma1<I.v * 1024>(src + 4096, ring0 + 4 * 8192 + 4096, voff); });
    }
    const char* src0 = keysm + (size_t)tile_of(0) * TILEB;
    const char* src1 = keysm + (size_t)tile_of(1) * TILEB;
    auto stream = [&](auto I) {
      if constexpr (I.v < 72) wload<I.v, false>(wf[I.v], voff, wa4);
      else if constexpr (I.v < 96) dma_k(IC<(I.v - 72) & 7>{}, src0 + ((I.v - 72) >> 3) * 8192, ring0 + ((I.v - 72) >> 3) * 8192);
      else dma_k(IC<(I.v - 96) & 7>{}, src1 + ((I.v - 96) >> 3) * 8192, ring0 + (3 + ((I.v - 96) >> 3)) * 8192);
    };
    auto stream_z = [&](auto I) {
      stream(I);
      if constexpr (I.v < 24) zero8<I.v & 1>(o[I.v >> 1]);
    };
    sfor<0, 12>(stream_z);
    STAMP(1);
    wait_vm<12>();
    asm volatile("s_barrier" ::: "memory");
    sfor<12, WIN>(stream_z);
    STAMP(2);
    f32x16 acc[3] = {};
    bf16x8 hf[24];
    auto hread = [&](auto I) {
      hf[I.v] = *(const bf16x8*)(lds + (I.v / 6) * 40960 + 4 * 8192 + (I.v % 6) * 1024 + ((I.v & 1) ? so1 : so0));
    };
    sfor<0, 2>(hread);
    SB();
    sfor<0, 24>([&](auto I) {
      constexpr int ks = I.v;
      if constexpr (ks + 2 < 24) hread(IC<ks + 2>{});
      constexpr int i0 = (ks < 14) ? 3 * ks + WIN : 72 + 2 * (ks - 14), i1 = (ks < 14) ? i0 + 3 : i0 + 2;
      sfor<i0, i1>(stream);
      wwait<i1 - 3 * ks - 3, false>(wf[3 * ks + 2]); wtie<false>(wf[3 * ks]); wtie<false>(wf[3 * ks + 1]);
#pragma unroll
      for (int i = 0; i < 3; ++i) acc[i] = MFMA(wf[3 * ks + i], hf[ks], acc[i]);
      SB();
    });
    STAMP(3);
    sfor<0, 3>([&](auto I) {
#pragma unroll
      for (int s = 0; s < 2; ++s) *(bf16x8*)(ring + 3 * 8192 + (2 * I.v + s) * 1024 + l * 16) = pack8(acc[I.v], s);
      SB();
      stream(IC<92 + I.v>{});
    });
    lds_barrier();
    sfor<0, 4>([&](auto I) {
#pragma unroll
      for (int u = 3 * I.v; u < 3 * I.v + 3; ++u) {
        const int dt = (u & ~3) + ((u & 3) ^ w), ow = dt / 3;
        const char* p = lds + ow * 40960 + 3 * 8192 + (dt - 3 * ow) * 2048 + l * 16;
        qf[2 * u] = *(const bf16x8*)p; qf[2 * u + 1] = *(const bf16x8*)(p + 1024);
      }
      SB();
      if constexpr (I.v == 0) stream(IC<95>{});
    });
    lds_barrier();
    STAMP(4);
    bf16x8 kf[26];
    auto kread = [&](auto I) {
      constexpr int f = I.v;
      if constexpr ((f & 1) == 0) wait_vm<23 + ((f > 2) ? (f - 2) / 2 : 0) - (f + 1)>();
      kf[f] = *(const bf16x8*)(ring + f * 1024 + ((f & 1) ? so1 : so0));
    };
    sfor<0, 2>(kread);
    sstart(scur, tile_of(0));
    SB();
    sfor<0, 24>([&](auto I) {
      constexpr int f = I.v;
      kread(IC<f + 2>{});
      if constexpr (f & 1) stream(IC<96 + f / 2>{});
      if constexpr (f < 23) mfma_s(scur, kf[f], qf[f]); else mfma_s_end(scur, kf[f], qf[f]);
      SB();
    });
    kc[0] = kf[24]; kc[1] = kf[25];
  }

  const int blk = (l >> 4) & 1, q4 = (l & 15) >> 2, p4 = l & 3, hh = p4 & 1;
  const int xr4 = 4 * (2 * blk + hh);
  const int to0 = blk * 1024 + (hh * 32 + ((4 * h + q4) ^ xr4)) * 16 + 8 * (p4 >> 1);
  const int to1 = blk * 1024 + (hh * 32 + ((8 + 4 * h + q4) ^ xr4)) * 16 + 8 * (p4 >> 1);

  float mrun = 0.f;
  f32x2 lsum = {0.f, 0.f};
  bf16x8 w2[72];
  auto w2issue = [&](auto I) { if constexpr (I.v < 72) wload<I.v, w2a(I.v)>(w2[I.v], voff, wb4); };

  int sg0 = 0, sg1 = 8192, sg2 = 16384, sg3 = 24576, sg4 = 32768;
  const char* srcA = keysm + (size_t)tile_of(1) * TILEB;
  const char* srcB = keysm + (size_t)tile_of(2) * TILEB;
  auto period = [&](auto MODEC, int jj) {
    constexpr int MODE = MODEC.v;
    constexpr int PFD = 4;
    char* rb[3] = {ring + sg0, ring + sg1, ring + sg2};
    char* nb[4] = {ring + sg3, ring + sg4, ring + sg0, ring + sg1};
    auto stream = [&](auto S) {
      constexpr int e = ev_at(S.v);
      if constexpr (MODE == 2) {
        constexpr int sl = S.v, i0 = (sl < 8) ? sl : (sl < 24) ? 8 + (sl - 8) + (sl - 7) / 2 : 8 + sl;
        if constexpr (sl < 32) w2issue(IC<12 + i0>{});
        if constexpr (sl >= 8 && sl < 24 && (sl & 1) == 0) w2issue(IC<13 + i0>{});
      } else if constexpr (e >= 0) {
        if constexpr (e < 4) dma_k(IC<4 + e>{}, srcA + 8192, ring0 + sg4);
        else if constexpr (e < 12) dma_k(IC<e - 4>{}, srcA + 16384, ring0 + sg0);
        else if constexpr (MODE == 1) w2issue(IC<e - 12>{});
        else if constexpr (e < 20) dma_k(IC<e - 12>{}, srcB, ring0 + sg1);
        else dma_k(IC<e - 20>{}, srcB + 8192, ring0 + sg2);
      }
    };
    bf16x8 kf[26], tf[24];
    kf[0] = kc[0]; kf[1] = kc[1];
    auto kread = [&](auto I, auto S) {
      constexpr int f = I.v;
      if constexpr (MODE == 0 || (MODE == 1 && f < 24)) {
        if constexpr ((f & 1) == 0) wait_vm<ev_before(S.v) - 1 - (f + 1 - 12)>();
        kf[f] = *(const bf16x8*)(nb[f >> 3] + (f & 7) * 1024 + ((f & 1) ? so1 : so0));
      }
    };
    auto pv_u = [](int p) { return p < 8 ? (p & 3) : (p >> 1); };
    auto pv_s = [](int p) { return p < 8 ? (p >> 2) : (p & 1); };
    auto tread = [&](auto I) {
      constexpr int u = pv_u(I.v);
      const char* b = rb[u >> 2] + (u & 3) * 2048 + pv_s(I.v) * 256;
      tf[I.v] = tr_frag(b + to0, b + to1);
    };
    float mx;
    if constexpr (MODE != 2) sstart(snext, tile_of(jj + 1));
    sfor<0, 3>([&](auto I) {
      kread(IC<I.v + 2>{}, I);
      if constexpr (MODE != 2) mfma_s(snext, kf[I.v], qf[I.v]);
      if constexpr (I.v == 1) mx = max3(max3(max3(max3(scur[0], scur[1], scur[2]), scur[3], scur[4]), scur[5], scur[6]), scur[7], scur[8]);
      if constexpr (I.v == 2) {
        mx = max3(max3(max3(max3(mx, scur[9], scur[10]), scur[11], scur[12]), scur[13], scur[14]), scur[15], scur[15]);
        const unsigned u = __builtin_bit_cast(unsigned, mx);
        const auto sw = __builtin_amdgcn_permlane32_swap(u, u, false, false);
        mx = max3(__builtin_bit_cast(float, (unsigned)sw[0]), __builtin_bit_cast(float, (unsigned)sw[1]), mx);
      }
      stream(I);
      SB();
    });
    if (jj == 0 || __any(mx > 64.f)) {
      const unsigned mb = pack2(mrun + ((jj == 0) ? mx : fmaxf(mx, 0.f)), 0.f) & 0xffffu;
      const float dl = bflo(mb) - mrun;
      mrun = bflo(mb);
      cb[0] = h ? 0u : (0xff610000u | (mb ^ 0x8000u));
#pragma unroll
      for (int i = 0; i < 16; ++i) scur[i] -= dl;
      if constexpr (MODE != 2) {
        asm volatile("s_nop 7\n\ts_nop 7\n\tv_sub_f32 %0, %0, %1" : "+v"(snext[0]) : "v"(dl));
#pragma unroll
        for (int i = 1; i < 15; ++i) asm volatile("v_sub_f32 %0, %0, %1" : "+v"(snext[i]) : "v"(dl));
        asm volatile("v_sub_f32 %0, %0, %1\n\ts_nop 1" : "+v"(snext[15]) : "v"(dl));
      }
      if (jj != 0) {
        const float f = __builtin_amdgcn_exp2f(-dl);
        lsum *= f;
#pragma unroll
        for (int c = 0; c < 12; ++c)
#pragma unroll
          for (int i = 0; i < 16; ++i) {
            float tmp;
            asm volatile("v_accvgpr_read_b32 %1, %0\n\tv_mul_f32 %1, %1, %2\n\tv_accvgpr_write_b32 %0, %1" : "+a"(o[c][i]), "=&v"(tmp) : "v"(f));
          }
        asm volatile("s_nop 4");
      }
    }
    SB();
    bf16x8 pf0, pf1;
    sfor<3, 8>([&](auto I) {
      constexpr int f = I.v;
      if constexpr (f + 2 < 8) kread(IC<f + 2>{}, I);
      if constexpr (f == 4) { tread(IC<1>{}); tread(IC<0>{}); }
      if constexpr (f == 6) { tread(IC<3>{}); tread(IC<2>{}); }
      if constexpr (MODE != 2) mfma_s(snext, kf[f], qf[f]);
      if constexpr (f < 7) {
        scur[2 * (f - 3)] = __builtin_amdgcn_exp2f(scur[2 * (f - 3)]); scur[2 * (f - 3) + 1] = __builtin_amdgcn_exp2f(scur[2 * (f - 3) + 1]);
      } else pf0 = pack8(scur, 0);
      stream(I);
      SB();
    });
    auto oput = [&](auto I) {
      constexpr int u = I.v, k = 3 * (u >> 2) + (u & 3) - 1;
      if constexpr ((u & 3) != 0) {
        char* dst = ((k < 4) ? nb[k >> 1] : rb[(k - 4) >> 1]) + (k & 1) * 4096 + l * 16;
#pragma unroll
        for (int g = 0; g < 4; ++g) *(f32x4*)(dst + g * 1024) = f32x4{o[u][4 * g], o[u][4 * g + 1], o[u][4 * g + 2], o[u][4 * g + 3]};
      }
    };
    const char* srcC = srcB;
    sfor<0, 24>([&](auto I) {
      constexpr int p = I.v;
      if constexpr ((p & 1) == 0 && p + PFD < 24) { tread(IC<p + PFD + 1>{}); tread(IC<p + PFD>{}); }
      if constexpr (p >= 22) kread(IC<p - 14>{}, IC<8 + p>{});
      o[pv_u(p)] = MFMA(tf[p], pv_s(p) ? pf1 : pf0, o[pv_u(p)]);
      if constexpr (p < 4) {
        scur[8 + 2 * p] = __builtin_amdgcn_exp2f(scur[8 + 2 * p]); scur[9 + 2 * p] = __builtin_amdgcn_exp2f(scur[9 + 2 * p]);
        if constexpr (p == 3) pf1 = pack8(scur, 1);
      } else if constexpr (p < 12) lsum += f32x2{scur[2 * (p - 4)], scur[2 * (p - 4) + 1]};
      if constexpr (MODE == 0 && p == 13) srcC = keysm + (size_t)tile_of(jj + 3) * TILEB;
      stream(IC<8 + p>{});
      if constexpr (MODE == 2 && p >= 7 && p < 10) oput(IC<p - 6>{});
      if constexpr (MODE == 2 && p >= 13 && (p & 1)) oput(IC<(p - 3) / 2>{});
      SB();
    });
    if constexpr (MODE == 2) oput(IC<11>{});
    else {
      sfor<8, 24>([&](auto I) {
        constexpr int f = I.v;
        kread(IC<f + 2>{}, IC<24 + f>{});
        if constexpr (f < 23) { mfma_s(snext, kf[f], qf[f]); stream(IC<24 + f>{}); }
        else { stream(IC<24 + f>{}); mfma_s_move<MODE == 1>(scur, snext, kf[f], qf[f]); }
        SB();
      });
      if constexpr (MODE == 0) { kc[0] = kf[24]; kc[1] = kf[25]; }
      const int t0 = sg0, t1 = sg1, t2 = sg2;
      sg0 = sg3; sg1 = sg4; sg2 = t0; sg3 = t1; sg4 = t2;
      srcA = srcB; srcB = srcC;
    }
  };
  {
    int jj = 0;
    do period(IC<0>{}, jj); while (++jj < nt - 2);
  }
  STAMP(5);
  period(IC<1>{}, nt - 2);
  STAMP(6);
  period(IC<2>{}, nt - 1);
  STAMP(7);

  const float ltot = xsum(lsum[0] + lsum[1]);
  auto oslot = [&](int sw, int k) {
    const int b = (sw < 3) ? 0 : 4;
    const int i = b + k + 6;
    return lds + sw * 40960 + ((i >= 10) ? i - 10 : i) * 4096;
  };
  if (h == 0) *(f32x2*)(oslot(w, 9) + r * 8) = f32x2{mrun, ltot};
  SB();
  sfor<52, 54>(w2issue);
  lds_barrier();
  float fs[4];
  {
    const f32x2 a = *(const f32x2*)(oslot(w, 9) + r * 8), b = *(const f32x2*)(oslot(w ^ 1, 9) + r * 8);
    const f32x2 c = *(const f32x2*)(oslot(w ^ 2, 9) + r * 8), d = *(const f32x2*)(oslot(w ^ 3, 9) + r * 8);
    const float m = fmaxf(fmaxf(a[0], b[0]), fmaxf(c[0], d[0]));
    fs[0] = __builtin_amdgcn_exp2f(a[0] - m); fs[1] = __builtin_amdgcn_exp2f(b[0] - m);
    fs[2] = __builtin_amdgcn_exp2f(c[0] - m); fs[3] = __builtin_amdgcn_exp2f(d[0] - m);
    const float inv = __builtin_amdgcn_rcpf(a[1] * fs[0] + b[1] * fs[1] + c[1] * fs[2] + d[1] * fs[3]);
    fs[0] *= inv; fs[1] *= inv; fs[2] *= inv; fs[3] *= inv;
  }
  f32x4 pa[4][3];
  u32x4 opk[2];
  auto mread = [&](auto SG, auto G) {
#pragma unroll
    for (int cc = 1; cc < 4; ++cc) pa[G.v][cc - 1] = *(const f32x4*)(oslot(w ^ cc, 3 * SG.v + cc - 1) + l * 16 + G.v * 1024);
  };
  auto mstep = [&](auto SG, auto G) {
    constexpr int sg = SG.v, g = G.v;
    f32x4 own;
#pragma unroll
    for (int e = 0; e < 4; ++e) {
      float tmp;
      asm volatile("v_accvgpr_read_b32 %0, %1" : "=v"(tmp) : "a"(o[4 * sg][4 * g + e]));
      own[e] = tmp;
    }
    own = own * fs[0] + pa[g][0] * fs[1] + pa[g][1] * fs[2] + pa[g][2] * fs[3];
    opk[g >> 1][2 * (g & 1)] = pack2(own[0], own[1]); opk[g >> 1][2 * (g & 1) + 1] = pack2(own[2], own[3]);
    if constexpr (g == 3) {
      char* dst = oslot(w ^ 1, 3 * sg) + l * 16;
      *(u32x4*)dst = opk[0]; *(u32x4*)(dst + 1024) = opk[1];
    }
  };
  sfor<0, 4>([&](auto G) { mread(IC<0>{}, G); });
  SB();
  sfor<54, 56>(w2issue);
  sfor<0, 4>([&](auto G) { mstep(IC<0>{}, G); });
  lds_barrier();
  STAMP(8);
  f32x16 acc[3] = {};
  u32x4 xv[6];
  f32x2 ms;
  {
    const char* x1 = H2 + ((size_t)t * NTILE + qt) * TILEB + (size_t)(6 * w) * 1024 + 2048;
    const char* xa = x1 + so0;
    const char* xb = x1 + so1;
    bf16x8 of[24];
    auto oread = [&](auto I) {
      constexpr int ks = I.v;
      of[ks] = *(const bf16x8*)(oslot(((ks >> 1) & 3) ^ 1, 3 * (ks >> 3)) + (ks & 1) * 1024 + l * 16);
    };
    sfor<0, 3>([&](auto SG) {
      constexpr int sg = SG.v;
      sfor<8 * sg, 8 * sg + 2>(oread);
      SB();
      sfor<0, 8>([&](auto K) {
        constexpr int k = K.v, ks = 8 * sg + k;
        if constexpr (k + 2 < 8) oread(IC<ks + 2>{});
        if constexpr (sg < 2 && k < 4) mread(IC<sg + 1>{}, K);
        if constexpr (ks < 8) sfor<56 + 2 * ks, 58 + 2 * ks>(w2issue);
        else if constexpr (ks < 14) gload_p<(ks - 8) * 1024 - 2048>(xv[ks - 8], ((ks - 8) & 1) ? xb : xa);
        else if constexpr (ks == 14)
          asm volatile("global_load_dwordx2 %0, %1, off" : "=v"(ms) : "v"(X1 + ((size_t)t * (32 * NTILE) + 32 * qt + r) * 8));
        constexpr int issued = (ks < 8) ? 58 + 2 * ks : ((ks < 15) ? 72 + (ks - 7) : 79);
        wwait<issued - 3 * ks - 3, w2a(3 * ks + 2)>(w2[3 * ks + 2]); wtie<w2a(3 * ks)>(w2[3 * ks]); wtie<w2a(3 * ks + 1)>(w2[3 * ks + 1]);
#pragma unroll
        for (int i = 0; i < 3; ++i) acc[i] = MFMA(w2[3 * ks + i], of[ks], acc[i]);
        if constexpr (sg < 2 && k >= 2 && k < 6) mstep(IC<sg + 1>{}, IC<k - 2>{});
        SB();
      });
      if constexpr (sg < 2) lds_barrier();
    });
  }
  STAMP(9);
  wait6<0>(xv);
  asm volatile("" : "+v"(ms));
  {
    char* ob0 = oslot(w ^ 2, 1);
    char* ob1 = oslot(w ^ 3, 2) - 16 * 208;
#pragma unroll
    for (int i = 0; i < 6; ++i) {
      u32x4 p;
#pragma unroll
      for (int j = 0; j < 4; ++j)
        p[j] = pk(unpk(xv[i][j]) * ms[1] + (ms[0] + f32x2{acc[i >> 1][8 * (i & 1) + 2 * j], acc[i >> 1][8 * (i & 1) + 2 * j + 1]}));
      *(u32x4*)(((r < 16) ? ob0 : ob1) + r * 208 + i * 32 + h * 16) = p;
    }
    unsigned short* obase = out + ((size_t)t * NP + 32 * qt) * CH + 96 * w;
#pragma unroll
    for (int i = 0; i < 6; ++i) {
      const int id = i * 64 + l, row = id / 12, ch = id - 12 * row;
      const u32x4 p = *(const u32x4*)(((i < 3) ? ob0 : ob1) + row * 208 + ch * 16);
      if (32 * qt + row < NP) *(u32x4*)(obase + (size_t)row * CH + 8 * ch) = p;
    }
  }
  STAMP(10);
  STOUT((char*)X1 + (size_t)TF * NTILE * TILEB + 1048576);
}

extern "C" void kernel_launch(void* const* d_in, const int* in_sizes, int n_in,
                              void* d_out, int out_size, void* d_ws, size_t ws_size,
                              hipStream_t stream) {
  const unsigned short* x = (const unsigned short*)d_in[0];
  const unsigned short* wqkv_t = (const unsigned short*)d_in[1];
  const unsigned short* wo_t = (const unsigned short*)d_in[2];
  const unsigned short* wqkv_s = (const unsigned short*)d_in[3];
  const unsigned short* wo_s = (const unsigned short*)d_in[4];
  char* ws = (char*)d_ws;
  k0_fold<<<144, 256, 0, stream>>>(wqkv_t, wo_t, wqkv_s, wo_s, ws);
  k1_temporal<<<184, 768, 0, stream>>>(x, ws + OFF_WA_T, ws + OFF_WB_T, ws + OFF_H2, ws + OFF_X1);
  k2_spatial<<<184, 256, 0, stream>>>(ws + OFF_WA_S, ws + OFF_WB_S, ws + OFF_H2, ws + OFF_X1, (unsigned short*)d_out);
}
```

```cpp
#include <hip/hip_runtime.h>
#include <hip/hip_bf16.h>

using bf16x8 = __attribute__((ext_vector_type(8))) __bf16;
using bf16x2 = __attribute__((ext_vector_type(2))) __bf16;
using s16x4  = __attribute__((ext_vector_type(4))) short;
using s16x8  = __attribute__((ext_vector_type(8))) short;
using f32x16 = __attribute__((ext_vector_type(16))) float;
using f32x4  = __attribute__((ext_vector_type(4))) float;
using f32x2  = __attribute__((ext_vector_type(2))) float;
using u32x4  = __attribute__((ext_vector_type(4))) unsigned;
typedef __attribute__((address_space(3))) s16x4 lds_s16x4;

#define DI __device__ __forceinline__
#define MFMA(a, b, c) __builtin_amdgcn_mfma_f32_32x32x16_bf16((a), (b), (c), 0, 0, 0)

constexpr int TF = 7, NP = 729, CH = 384;
constexpr int NTILE = 23;
constexpr int WBYTES = 12 * 24 * 1024;
constexpr int TILEB = 24 * 1024;
constexpr size_t OFF_WA_T = 0, OFF_WB_T = WBYTES, OFF_WA_S = 2 * WBYTES, OFF_WB_S = 3 * WBYTES;
constexpr size_t OFF_H2 = 4 * (size_t)WBYTES;
constexpr size_t OFF_X1 = OFF_H2 + (size_t)TF * NTILE * TILEB;

DI int perm23(int r) { return (r & 0x13) | ((r & 4) << 1) | ((r & 8) >> 1); }
DI int slot_off(int ksodd, int h, int r) { return (h * 32 + (r ^ (4 * (2 * ksodd + h)))) * 16; }

DI unsigned pack2(float a, float b) {
  f32x2 p = {a, b};
  return __builtin_bit_cast(unsigned, __builtin_convertvector(p, bf16x2));
}
DI bf16x8 pack8(const f32x16& x, int s) {
  u32x4 p;
  p[0] = pack2(x[8 * s + 0], x[8 * s + 1]); p[1] = pack2(x[8 * s + 2], x[8 * s + 3]);
  p[2] = pack2(x[8 * s + 4], x[8 * s + 5]); p[3] = pack2(x[8 * s + 6], x[8 * s + 7]);
  return __builtin_bit_cast(bf16x8, p);
}
DI float bflo(unsigned u) { return __builtin_bit_cast(float, u << 16); }
DI float bfhi(unsigned u) { return __builtin_bit_cast(float, u & 0xffff0000u); }
DI f32x2 unpk(unsigned u) { return f32x2{bflo(u), bfhi(u)}; }
DI unsigned pk(f32x2 p) { return __builtin_bit_cast(unsigned, __builtin_convertvector(p, bf16x2)); }
DI float xsum(float v) {
  const unsigned u = __builtin_bit_cast(unsigned, v);
  const auto r = __builtin_amdgcn_permlane32_swap(u, u, false, false);
  return __builtin_bit_cast(float, (unsigned)r[0]) + __builtin_bit_cast(float, (unsigned)r[1]);
}
DI float xmax(float v) {
  const unsigned u = __builtin_bit_cast(unsigned, v);
  const auto r = __builtin_amdgcn_permlane32_swap(u, u, false, false);
  return fmaxf(__builtin_bit_cast(float, (unsigned)r[0]), __builtin_bit_cast(float, (unsigned)r[1]));
}
DI void lds_barrier() { asm volatile("s_waitcnt lgkmcnt(0)\n\ts_barrier" ::: "memory"); }
#define SB() __builtin_amdgcn_sched_barrier(0)
template <int I> struct IC { static constexpr int v = I; };
template <int B, int E, class F> DI void sfor(F&& f) { if constexpr (B < E) { f(IC<B>{}); sfor<B + 1, E>(f); } }
template <int OFF> DI void gload_a(bf16x8& dst, unsigned voff, const char* sbase) {
  asm volatile("global_load_dwordx4 %0, %1, %2 offset:%3" : "=a"(dst) : "v"(voff), "s"(sbase), "i"(OFF));
}
template <int OFF> DI void gload_v(bf16x8& dst, unsigned voff, const char* sbase) {
  asm volatile("global_load_dwordx4 %0, %1, %2 offset:%3" : "=v"(dst) : "v"(voff), "s"(sbase), "i"(OFF));
}
template <int OFF> DI void gload_p(u32x4& dst, const void* p) {
  asm volatile("global_load_dwordx4 %0, %1, off offset:%2" : "=v"(dst) : "v"(p), "i"(OFF));
}
template <int IDX, bool AG> DI void wload(bf16x8& dst, unsigned voff, const char* base4k) {
  if constexpr (AG) gload_a<(IDX % 8) * 1024 - 4096>(dst, voff, base4k + (IDX / 8) * 8192);
  else gload_v<(IDX % 8) * 1024 - 4096>(dst, voff, base4k + (IDX / 8) * 8192);
}
template <int N, bool AG> DI void wwait(bf16x8& a) {
  constexpr int M = (N > 63) ? 63 : N;
  if constexpr (AG) asm volatile("s_waitcnt vmcnt(%1)" : "+a"(a) : "i"(M));
  else asm volatile("s_waitcnt vmcnt(%1)" : "+v"(a) : "i"(M));
}
template <bool AG> DI void wtie(bf16x8& a) {
  if constexpr (AG) asm volatile("" : "+a"(a)); else asm volatile("" : "+v"(a));
}
template <int N> DI void wait6(u32x4 (&x)[6]) {
  asm volatile("s_waitcnt vmcnt(%6)" : "+v"(x[0]), "+v"(x[1]), "+v"(x[2]), "+v"(x[3]), "+v"(x[4]), "+v"(x[5]) : "i"(N));
}

DI bf16x8 tr_frag(const char* p0, const char* p1) {
  s16x4 lo = __builtin_amdgcn_ds_read_tr16_b64_v4i16((lds_s16x4*)p0);
  s16x4 hi = __builtin_amdgcn_ds_read_tr16_b64_v4i16((lds_s16x4*)p1);
  s16x8 v = __builtin_shufflevector(lo, hi, 0, 1, 2, 3, 4, 5, 6, 7);
  return __builtin_bit_cast(bf16x8, v);
}

template <int IMM> DI void dma1(const char* srcm, unsigned m0v, unsigned voff) {
  asm volatile("s_mov_b32 m0, %1\n\ts_nop 0\n\tglobal_load_lds_dwordx4 %2, %0 offset:%3"
               :: "s"(srcm), "s"(m0v), "v"(voff), "i"(IMM) : "memory");
}
template <int N> DI void wait_vm() { asm volatile("s_waitcnt vmcnt(%0)" :: "i"(N) : "memory"); }

DI void gstore(void* p, u32x4 v) {
  asm volatile("global_store_dwordx4 %0, %1, off sc1\n\ts_nop 1" :: "v"(p), "v"(v) : "memory");
}
DI void dma1p(const void* p, unsigned m0v) {
  asm volatile("s_mov_b32 m0, %1\n\ts_nop 0\n\tglobal_load_lds_dwordx4 %0, off" :: "v"(p), "s"(m0v) : "memory");
}

__global__ __launch_bounds__(256) void k0_fold(const unsigned short* __restrict__ wqkv_t, const unsigned short* __restrict__ wo_t,
                                               const unsigned short* __restrict__ wqkv_s, const unsigned short* __restrict__ wo_s,
                                               char* __restrict__ ws) {
  __shared__ __attribute__((aligned(16))) char lds[2 * 49152];
  char* la = lds;
  char* lb = lds + 49152;
  const int b = blockIdx.x, prod = b / 36, rem = b % 36, nb = rem / 6, cb = rem % 6;
  const int tid = threadIdx.x, w = __builtin_amdgcn_readfirstlane(tid >> 6), l = tid & 63, r = l & 31, h = l >> 5;
  const unsigned short* wqkv = (prod < 2) ? wqkv_t : wqkv_s;
  const unsigned short* wo = (prod < 2) ? wo_t : wo_s;
  const bool isB = prod & 1;
  {
    const unsigned short* asrc = wqkv + (size_t)(64 * cb + 16 * w) * 1152 + (isB ? 768 : 0);
    const unsigned short* bsrc = wqkv + (size_t)(64 * nb + 16 * w) * 1152 + 384;
    const unsigned short* osrc = wo + (size_t)(96 * w + (l >> 3)) * 384 + 64 * nb + 8 * (l & 7);
    const unsigned la0 = (unsigned)(size_t)la + w * 12288, lb0 = (unsigned)(size_t)lb + w * 12288;
#pragma unroll
    for (int j = 0; j < 3; ++j) {
      const int id = 64 * j + l, row = id / 48, pos = id - 48 * row;
#pragma unroll
      for (int g = 0; g < 4; ++g) {
        const int rw = 4 * g + row;
        const int off = rw * 1152 + 8 * ((pos & ~15) | ((pos ^ rw) & 15));
        dma1p(asrc + off, la0 + (3 * g + j) * 1024);
        if (!isB) dma1p(bsrc + off, lb0 + (3 * g + j) * 1024);
        else dma1p(osrc + (size_t)(8 * (3 * g + j)) * 384, lb0 + (3 * g + j) * 1024);
      }
    }
  }
  const int ct = w & 1, nt = w >> 1;
  const int ra = 32 * ct + perm23(r), rb = 32 * nt + perm23(r);
  int aa[8], ab[8];
#pragma unroll
  for (int k = 0; k < 8; ++k) { aa[k] = ra * 768 + (((2 * k + h) ^ ra) & 15) * 16; ab[k] = rb * 768 + (((2 * k + h) ^ rb) & 15) * 16; }
  const int blk = (l >> 4) & 1, q = (l & 15) >> 2, p = l & 3;
  const char* tb = lb + (8 * h + q) * 128 + (32 * nt + 16 * blk + 8 * (p & 1) + 4 * (p >> 1)) * 2;
  asm volatile("s_waitcnt vmcnt(0)\n\ts_barrier" ::: "memory");
  f32x16 acc = {};
  bf16x8 af[24], bf[24];
  auto rd = [&](auto I, auto ISB) {
    constexpr int ks = I.v;
    af[ks] = *(const bf16x8*)(la + aa[ks & 7] + (ks >> 3) * 256);
    if constexpr (ISB.v) bf[ks] = tr_frag(tb + ks * 2048, tb + ks * 2048 + 512);
    else bf[ks] = *(const bf16x8*)(lb + ab[ks & 7] + (ks >> 3) * 256);
  };
  auto gemm = [&](auto ISB) {
    sfor<0, 4>([&](auto I) { rd(I, ISB); });
    SB();
    sfor<0, 24>([&](auto I) {
      if constexpr (I.v + 4 < 24) rd(IC<I.v + 4>{}, ISB);
      acc = MFMA(af[I.v], bf[I.v], acc);
      SB();
    });
  };
  if (!isB) {
    gemm(IC<0>{});
    const float sc = 0.05103103630798288f * 1.4426950408889634f;
#pragma unroll
    for (int i = 0; i < 16; ++i) acc[i] *= sc;
  } else gemm(IC<1>{});
  char* dst = ws + (size_t)prod * WBYTES;
  const int ntile = 2 * nb + nt, ctile = 2 * cb + ct;
#pragma unroll
  for (int s = 0; s < 2; ++s) {
    const int ks = 2 * ctile + s;
    const int idx = (prod < 2) ? ntile * 24 + ks : ((ntile / 3) * 24 + ks) * 3 + ntile % 3;
    gstore(dst + (size_t)idx * 1024 + l * 16, __builtin_bit_cast(u32x4, pack8(acc, s)));
  }
}

DI void partial_stats(const f32x2 (&v)[8], char* st, int r, int w, int h) {
  f32x2 s = v[0];
#pragma unroll
  for (int i = 1; i < 8; ++i) s += v[i];
  const float mean_w = xsum(s[0] + s[1]) * (1.f / 32.f);
  f32x2 q = {0.f, 0.f};
#pragma unroll
  for (int i = 0; i < 8; ++i) { const f32x2 d = v[i] - mean_w; q += d * d; }
  const float m2_w = xsum(q[0] + q[1]);
  *(float*)(st + r * 112 + h * 48 + w * 4) = h ? m2_w : mean_w;
}
DI void combine_stats(const char* st, int r, float& mean, float& rstd, float& vare) {
  f32x4 a[6];
#pragma unroll
  for (int i = 0; i < 6; ++i) a[i] = *(const f32x4*)(st + r * 112 + 16 * i);
  const f32x4 ms = a[0] + a[1] + a[2], qs = a[3] + a[4] + a[5];
  const float m = (ms[0] + ms[1] + ms[2] + ms[3]) * (1.f / 12.f);
  const f32x4 d0 = a[0] - m, d1 = a[1] - m, d2 = a[2] - m;
  const f32x4 e = qs + 32.f * (d0 * d0 + d1 * d1 + d2 * d2);
  mean = m;
  vare = (e[0] + e[1] + e[2] + e[3]) * (1.f / 384.f) + 1e-5f;
  rstd = __builtin_amdgcn_rsqf(vare);
}
template <int N> DI void wait2(u32x4 (&x)[2]) { asm volatile("s_waitcnt vmcnt(%2)" : "+v"(x[0]), "+v"(x[1]) : "i"(N)); }

__global__ __launch_bounds__(768, 3) void k1_temporal(const unsigned short* __restrict__ x, const char* __restrict__ WA,
                                                      const char* __restrict__ WB, char* __restrict__ H2, char* __restrict__ X1) {
  __shared__ __attribute__((aligned(16))) char lds[2 * TILEB + 2048 + 2 * 3584];
  char* himg = lds;
  char* qimg = lds + TILEB;
  char* amix = lds + 2 * TILEB;
  char* st1 = lds + 2 * TILEB + 2048;
  char* st2 = st1 + 3584;
  const int tid = threadIdx.x, w = __builtin_amdgcn_readfirstlane(tid >> 6), l = tid & 63, r = l & 31, h = l >> 5;
  const int pos = r & 3, t = r >> 2, n = 4 * blockIdx.x + pos;
  const bool valid = (r < 28) && (n < NP);
  asm volatile("" :: "s"(H2), "s"(X1), "s"(WB));
  bf16x8 wf[48];
  const unsigned voff = l * 16;
  const char* wa4 = WA + (size_t)w * 24 * 1024 + 4096;
  const char* wb4 = WB + (size_t)w * 24 * 1024 + 4096;
  auto wissue = [&](auto I) {
    constexpr int f = I.v;
    if constexpr (f < 24) wload<f, false>(wf[f], voff, wa4);
    else if constexpr (f < 48) wload<f - 24, false>(wf[f], voff, wb4);
  };
  const int so0 = slot_off(0, h, r), so1 = slot_off(1, h, r);
  u32x4 xr[2];
  {
#pragma unroll
    for (int i = 0; i < 2; ++i) {
      const int id = (2 * w + i) * 64 + l, row0 = id / 48, ps = id - 48 * row0;
      const int ch = (ps & ~15) | ((ps ^ row0) & 15);
      const int row = (row0 < 28) ? row0 : 27, nn = min(4 * (int)blockIdx.x + (row & 3), NP - 1);
      dma1p(x + ((size_t)(row >> 2) * NP + nn) * CH + 8 * ch, (unsigned)(size_t)qimg + (2 * w + i) * 1024);
    }
    asm volatile("s_barrier" ::: "memory");
    sfor<0, 4>(wissue);
    wait_vm<4>();
    asm volatile("s_barrier" ::: "memory");
    const char* xp = qimg + r * 768 + (w >> 2) * 256;
    xr[0] = *(const u32x4*)(xp + (((4 * w + h) ^ r) & 15) * 16); xr[1] = *(const u32x4*)(xp + (((4 * w + 2 + h) ^ r) & 15) * 16);
#pragma unroll
    for (int i = 0; i < 2; ++i) if (!valid) xr[i] = u32x4{0, 0, 0, 0};
    f32x2 v[8];
#pragma unroll
    for (int i = 0; i < 8; ++i) v[i] = unpk(xr[i >> 2][i & 3]);
    partial_stats(v, st1, r, w, h);
    SB();
    sfor<4, 9>(wissue);
    lds_barrier();
    float mean, rstd, vare;
    combine_stats(st1, r, mean, rstd, vare);
    const float nb = -mean * rstd;
#pragma unroll
    for (int i = 0; i < 2; ++i) {
      u32x4 p;
#pragma unroll
      for (int j = 0; j < 4; ++j) p[j] = pk(v[4 * i + j] * rstd + nb);
      *(u32x4*)(himg + (2 * w + i) * 1024 + (i ? so1 : so0)) = p;
    }
    SB();
    sfor<9, 14>(wissue);
    lds_barrier();
  }
  f32x16 acc = {};
  {
    bf16x8 hf[24];
    hf[0] = *(const bf16x8*)(himg + so0); hf[1] = *(const bf16x8*)(himg + 1024 + so1);
    SB();
    sfor<0, 24>([&](auto I) {
      constexpr int ks = I.v;
      if constexpr (ks + 2 < 24) hf[ks + 2] = *(const bf16x8*)(himg + (ks + 2) * 1024 + ((ks & 1) ? so1 : so0));
      constexpr int iss = (ks < 10) ? 15 + ks : 24;
      if constexpr (ks < 10) wissue(IC<iss - 1>{});
      wwait<iss - 1 - ks, false>(wf[ks]);
      acc = MFMA(wf[ks], hf[ks], acc);
      SB();
    });
  }
  *(bf16x8*)(qimg + (2 * w) * 1024 + l * 16) = pack8(acc, 0);
  *(bf16x8*)(qimg + (2 * w + 1) * 1024 + l * 16) = pack8(acc, 1);
  lds_barrier();
  if (w == 0) {
    f32x16 sc;
#pragma unroll
    for (int i = 0; i < 16; ++i) {
      const int row = (i & 3) + 8 * (i >> 2) + 4 * h;
      sc[i] = ((i & 3) == pos && row < 28) ? 0.f : -3.0e38f;
    }
    bf16x8 ha[24], qb[24];
    sfor<0, 5>([&](auto I) {
      ha[I.v] = *(const bf16x8*)(himg + I.v * 1024 + ((I.v & 1) ? so1 : so0));
      qb[I.v] = *(const bf16x8*)(qimg + I.v * 1024 + l * 16);
    });
    SB();
    sfor<0, 24>([&](auto I) {
      constexpr int ks = I.v;
      if constexpr (ks + 5 < 24) {
        ha[ks + 5] = *(const bf16x8*)(himg + (ks + 5) * 1024 + (((ks + 5) & 1) ? so1 : so0));
        qb[ks + 5] = *(const bf16x8*)(qimg + (ks + 5) * 1024 + l * 16);
      }
      sc = MFMA(ha[ks], qb[ks], sc);
      SB();
    });
    float mx = sc[0];
#pragma unroll
    for (int i = 1; i < 16; ++i) mx = fmaxf(mx, sc[i]);
    mx = xmax(mx);
    float sum = 0.f;
#pragma unroll
    for (int i = 0; i < 16; ++i) { sc[i] = __builtin_amdgcn_exp2f(sc[i] - mx); sum += sc[i]; }
    const float inv = __builtin_amdgcn_rcpf(xsum(sum));
#pragma unroll
    for (int i = 0; i < 16; ++i) sc[i] *= inv;
    *(bf16x8*)(amix + l * 16) = pack8(sc, 0);
    *(bf16x8*)(amix + 1024 + l * 16) = pack8(sc, 1);
    lds_barrier();
  }
  SB();
  sfor<24, 34>(wissue);
  if (w != 0) lds_barrier();
  {
    const int blk = (l >> 4) & 1, q = (l & 15) >> 2, p = l & 3, hh = p & 1;
    const int xr4 = 4 * (2 * blk + hh);
    const char* tb0 = himg + w * 2048 + blk * 1024 + (hh * 32 + ((4 * h + q) ^ xr4)) * 16 + 8 * (p >> 1);
    const char* tb1 = himg + w * 2048 + blk * 1024 + (hh * 32 + ((8 + 4 * h + q) ^ xr4)) * 16 + 8 * (p >> 1);
    f32x16 a = {};
    a = MFMA(tr_frag(tb0, tb1), *(const bf16x8*)(amix + l * 16), a);
    a = MFMA(tr_frag(tb0 + 256, tb1 + 256), *(const bf16x8*)(amix + 1024 + l * 16), a);
    *(bf16x8*)(qimg + (2 * w) * 1024 + l * 16) = pack8(a, 0);
    *(bf16x8*)(qimg + (2 * w + 1) * 1024 + l * 16) = pack8(a, 1);
  }
  SB();
  sfor<34, 36>(wissue);
  lds_barrier();
  acc = f32x16{};
  {
    bf16x8 hf[24];
    hf[0] = *(const bf16x8*)(qimg + l * 16); hf[1] = *(const bf16x8*)(qimg + 1024 + l * 16);
    SB();
    sfor<0, 24>([&](auto I) {
      constexpr int ks = I.v;
      if constexpr (ks + 2 < 24) hf[ks + 2] = *(const bf16x8*)(qimg + (ks + 2) * 1024 + l * 16);
      if constexpr (ks < 12) wissue(IC<36 + ks>{});
      wwait<((ks < 12) ? 37 + ks : 48) - 1 - (24 + ks), false>(wf[24 + ks]);
      acc = MFMA(wf[24 + ks], hf[ks], acc);
      SB();
    });
  }
  {
    f32x2 v[8];
#pragma unroll
    for (int i = 0; i < 8; ++i) v[i] = unpk(pk(unpk(xr[i >> 2][i & 3]) + f32x2{acc[2 * i], acc[2 * i + 1]}));
    partial_stats(v, st2, r, w, h);
    const int rr = n & 31;
    const size_t tile = ((size_t)t * NTILE + (n >> 5)) * TILEB + (size_t)(2 * w) * 1024;
    const int d0 = slot_off(0, h, rr), d1 = slot_off(1, h, rr);
    lds_barrier();
    float mean, rstd, vare;
    combine_stats(st2, r, mean, rstd, vare);
    const float nb = -mean * rstd;
    if (r < 28) {
#pragma unroll
      for (int i = 0; i < 2; ++i) {
        u32x4 p;
#pragma unroll
        for (int j = 0; j < 4; ++j) p[j] = pk(v[4 * i + j] * rstd + nb);
        gstore(H2 + tile + i * 1024 + (i ? d1 : d0), p);
      }
      if (w == 0 && h == 0) *(f32x2*)(X1 + ((size_t)t * (32 * NTILE) + n) * 8) = f32x2{mean, vare * rstd};
    }
  }
}

constexpr bool w2a(int f) { return f < 16 || f >= 52; }
DI void mfma_s(f32x16& acc, const bf16x8& a, const bf16x8& b) {
  asm volatile("v_mfma_f32_32x32x16_bf16 %0, %1, %2, %0" : "+v"(acc) : "v"(a), "v"(b));
}
DI void mfma_s_end(f32x16& acc, const bf16x8& a, const bf16x8& b) {
  asm volatile("v_mfma_f32_32x32x16_bf16 %0, %1, %2, %0\n\ts_nop 7\n\ts_nop 7" : "+v"(acc) : "v"(a), "v"(b));
}
template <bool NOPS> DI void mfma_s_move(f32x16& dst, const f32x16& acc, const bf16x8& a, const bf16x8& b) {
  if constexpr (NOPS) asm volatile("v_mfma_f32_32x32x16_bf16 %0, %2, %3, %1\n\ts_nop 7\n\ts_nop 7" : "=&v"(dst) : "v"(acc), "v"(a), "v"(b));
  else asm volatile("v_mfma_f32_32x32x16_bf16 %0, %2, %3, %1" : "=&v"(dst) : "v"(acc), "v"(a), "v"(b));
}
template <int HALF> DI void zero8(f32x16& a) {
#pragma unroll
  for (int i = 0; i < 8; ++i) asm volatile("v_accvgpr_write_b32 %0, 0" : "=a"(a[8 * HALF + i]));
}
DI void mfma_s0(f32x16& acc, const bf16x8& a, const bf16x8& b) {
  asm volatile("s_nop 1\n\tv_mfma_f32_32x32x16_bf16 %0, %1, %2, 0" : "=&v"(acc) : "v"(a), "v"(b));
}
DI float max3(float a, float b, float c) { float r; asm volatile("v_max3_f32 %0, %1, %2, %3" : "=v"(r) : "v"(a), "v"(b), "v"(c)); return r; }
constexpr int ev_slot(int e) { return e < 4 ? 2 * e + 1 : e < 20 ? 16 + 3 * ((e - 4) / 2) + ((e - 4) & 1) : 41 + 2 * (e - 20); }
constexpr int ev_at(int slot) { for (int e = 0; e < 24; ++e) if (ev_slot(e) == slot) return e; return -1; }
constexpr int ev_before(int slot) { int n = 0; for (int e = 0; e < 24; ++e) n += ev_slot(e) < slot; return n; }

__global__ __launch_bounds__(256, 1) void k2_spatial(const char* __restrict__ WA, const char* __restrict__ WB,
                                                     const char* __restrict__ H2, const char* __restrict__ X1,
                                                     unsigned short* __restrict__ out) {
  __shared__ __attribute__((aligned(16))) char lds[163840];
  const int t = blockIdx.x & 7, qt = blockIdx.x >> 3;
  if (t == 7) return;
  const int tid = threadIdx.x, w = __builtin_amdgcn_readfirstlane(tid >> 6), l = tid & 63, r = l & 31, h = l >> 5;
  asm volatile("" :: "s"(WB), "s"(X1), "s"(out));
  const unsigned voff = l * 16;
  const int so0 = slot_off(0, h, r), so1 = slot_off(1, h, r);
  unsigned vu[4];
  vu[0] = voff + (0 ^ w) * 2048 + 2048; vu[1] = voff + (1 ^ w) * 2048;
  vu[2] = voff + (2 ^ w) * 2048 + 2048; vu[3] = voff + (3 ^ w) * 2048;
  const char* keysm = H2 + (size_t)t * NTILE * TILEB - 2048;
  const int nt = (w < 3) ? 6 : 5;
  char* ring = lds + w * 40960;
  const unsigned ring0 = (unsigned)(size_t)ring;
  const char* wa4 = WA + (size_t)w * 72 * 1024 + 4096;
  const char* wb4 = WB + (size_t)w * 72 * 1024 + 4096;
  auto tile_of = [&](int jj) { const int i = 4 * jj + w + qt; return (i >= NTILE) ? i - NTILE : i; };
  auto dma_k = [&](auto K, const char* segsrc, unsigned m) {
    constexpr int k = K.v, cc = k >> 1;
    dma1<(k & 3) * 1024>(segsrc, m + (cc >= 2 ? 4096 : 0), vu[cc]);
  };
  u32x4 cb = {h ? 0u : 0xff610000u, 0u, 0u, 0u};
  auto sstart = [&](f32x16& sa, int tile) {
    const bool msk = (tile == NTILE - 1) && (r >= NP - 32 * (NTILE - 1));
    const u32x4 ca = {h ? 0u : (msk ? 0x3f803f80u : 0x3f80u), h ? 0u : 0x3f80u, 0u, 0u};
    mfma_s0(sa, __builtin_bit_cast(bf16x8, ca), __builtin_bit_cast(bf16x8, cb));
  };

  bf16x8 qf[24];
  f32x16 scur, snext;
  f32x16 o[12];
  bf16x8 kc[2];
  {
    constexpr int WIN = 30;
    bf16x8 wf[72];
    {
      const char* src = keysm + 2048 + (size_t)qt * TILEB + w * 6144;
      sfor<0, 4>([&](auto I) { dma1<I.v * 1024>(src, ring0 + 4 * 8192, voff); });
      sfor<0, 2>([&](auto I) { dma1<I.v * 1024>(src + 4096, ring0 + 4 * 8192 + 4096, voff); });
    }
    const char* src0 = keysm + (size_t)tile_of(0) * TILEB;
    const char* src1 = keysm + (size_t)tile_of(1) * TILEB;
    auto stream = [&](auto I) {
      if constexpr (I.v < 72) wload<I.v, false>(wf[I.v], voff, wa4);
      else if constexpr (I.v < 96) dma_k(IC<(I.v - 72) & 7>{}, src0 + ((I.v - 72) >> 3) * 8192, ring0 + ((I.v - 72) >> 3) * 8192);
      else dma_k(IC<(I.v - 96) & 7>{}, src1 + ((I.v - 96) >> 3) * 8192, ring0 + (3 + ((I.v - 96) >> 3)) * 8192);
    };
    auto stream_z = [&](auto I) {
      stream(I);
      if constexpr (I.v < 24) zero8<I.v & 1>(o[I.v >> 1]);
    };
    sfor<0, 12>(stream_z);
    wait_vm<12>();
    asm volatile("s_barrier" ::: "memory");
    sfor<12, WIN>(stream_z);
    f32x16 acc[3] = {};
    bf16x8 hf[24];
    auto hread = [&](auto I) {
      hf[I.v] = *(const bf16x8*)(lds + (I.v / 6) * 40960 + 4 * 8192 + (I.v % 6) * 1024 + ((I.v & 1) ? so1 : so0));
    };
    sfor<0, 2>(hread);
    SB();
    sfor<0, 24>([&](auto I) {
      constexpr int ks = I.v;
      if constexpr (ks + 2 < 24) hread(IC<ks + 2>{});
      constexpr int i0 = (ks < 14) ? 3 * ks + WIN : 72 + 2 * (ks - 14), i1 = (ks < 14) ? i0 + 3 : i0 + 2;
      sfor<i0, i1>(stream);
      wwait<i1 - 3 * ks - 3, false>(wf[3 * ks + 2]); wtie<false>(wf[3 * ks]); wtie<false>(wf[3 * ks + 1]);
#pragma unroll
      for (int i = 0; i < 3; ++i) acc[i] = MFMA(wf[3 * ks + i], hf[ks], acc[i]);
      SB();
    });
    sfor<0, 3>([&](auto I) {
#pragma unroll
      for (int s = 0; s < 2; ++s) *(bf16x8*)(ring + 3 * 8192 + (2 * I.v + s) * 1024 + l * 16) = pack8(acc[I.v], s);
      SB();
      stream(IC<92 + I.v>{});
    });
    lds_barrier();
    sfor<0, 4>([&](auto I) {
#pragma unroll
      for (int u = 3 * I.v; u < 3 * I.v + 3; ++u) {
        const int dt = (u & ~3) + ((u & 3) ^ w), ow = dt / 3;
        const char* p = lds + ow * 40960 + 3 * 8192 + (dt - 3 * ow) * 2048 + l * 16;
        qf[2 * u] = *(const bf16x8*)p; qf[2 * u + 1] = *(const bf16x8*)(p + 1024);
      }
      SB();
      if constexpr (I.v == 0) stream(IC<95>{});
    });
    lds_barrier();
    bf16x8 kf[26];
    auto kread = [&](auto I) {
      constexpr int f = I.v;
      if constexpr ((f & 1) == 0) wait_vm<23 + ((f > 2) ? (f - 2) / 2 : 0) - (f + 1)>();
      kf[f] = *(const bf16x8*)(ring + f * 1024 + ((f & 1) ? so1 : so0));
    };
    sfor<0, 2>(kread);
    sstart(scur, tile_of(0));
    SB();
    sfor<0, 24>([&](auto I) {
      constexpr int f = I.v;
      kread(IC<f + 2>{});
      if constexpr (f & 1) stream(IC<96 + f / 2>{});
      if constexpr (f < 23) mfma_s(scur, kf[f], qf[f]); else mfma_s_end(scur, kf[f], qf[f]);
      SB();
    });
    kc[0] = kf[24]; kc[1] = kf[25];
  }

  const int blk = (l >> 4) & 1, q4 = (l & 15) >> 2, p4 = l & 3, hh = p4 & 1;
  const int xr4 = 4 * (2 * blk + hh);
  const int to0 = blk * 1024 + (hh * 32 + ((4 * h + q4) ^ xr4)) * 16 + 8 * (p4 >> 1);
  const int to1 = blk * 1024 + (hh * 32 + ((8 + 4 * h + q4) ^ xr4)) * 16 + 8 * (p4 >> 1);

  float mrun = 0.f;
  f32x2 lsum = {0.f, 0.f};
  bf16x8 w2[72];
  auto w2issue = [&](auto I) { if constexpr (I.v < 72) wload<I.v, w2a(I.v)>(w2[I.v], voff, wb4); };

  int sg0 = 0, sg1 = 8192, sg2 = 16384, sg3 = 24576, sg4 = 32768;
  const char* srcA = keysm + (size_t)tile_of(1) * TILEB;
  const char* srcB = keysm + (size_t)tile_of(2) * TILEB;
  auto period = [&](auto MODEC, int jj) {
    constexpr int MODE = MODEC.v;
    constexpr int PFD = 4;
    char* rb[3] = {ring + sg0, ring + sg1, ring + sg2};
    char* nb[4] = {ring + sg3, ring + sg4, ring + sg0, ring + sg1};
    auto stream = [&](auto S) {
      constexpr int e = ev_at(S.v);
      if constexpr (MODE == 2) {
        constexpr int sl = S.v, i0 = (sl < 8) ? sl : (sl < 24) ? 8 + (sl - 8) + (sl - 7) / 2 : 8 + sl;
        if constexpr (sl < 32) w2issue(IC<12 + i0>{});
        if constexpr (sl >= 8 && sl < 24 && (sl & 1) == 0) w2issue(IC<13 + i0>{});
      } else if constexpr (e >= 0) {
        if constexpr (e < 4) dma_k(IC<4 + e>{}, srcA + 8192, ring0 + sg4);
        else if constexpr (e < 12) dma_k(IC<e - 4>{}, srcA + 16384, ring0 + sg0);
        else if constexpr (MODE == 1) w2issue(IC<e - 12>{});
        else if constexpr (e < 20) dma_k(IC<e - 12>{}, srcB, ring0 + sg1);
        else dma_k(IC<e - 20>{}, srcB + 8192, ring0 + sg2);
      }
    };
    bf16x8 kf[26], tf[24];
    kf[0] = kc[0]; kf[1] = kc[1];
    auto kread = [&](auto I, auto S) {
      constexpr int f = I.v;
      if constexpr (MODE == 0 || (MODE == 1 && f < 24)) {
        if constexpr ((f & 1) == 0) wait_vm<ev_before(S.v) - 1 - (f + 1 - 12)>();
        kf[f] = *(const bf16x8*)(nb[f >> 3] + (f & 7) * 1024 + ((f & 1) ? so1 : so0));
      }
    };
    auto pv_u = [](int p) { return p < 8 ? (p & 3) : (p >> 1); };
    auto pv_s = [](int p) { return p < 8 ? (p >> 2) : (p & 1); };
    auto tread = [&](auto I) {
      constexpr int u = pv_u(I.v);
      const char* b = rb[u >> 2] + (u & 3) * 2048 + pv_s(I.v) * 256;
      tf[I.v] = tr_frag(b + to0, b + to1);
    };
    float mx;
    if constexpr (MODE != 2) sstart(snext, tile_of(jj + 1));
    sfor<0, 3>([&](auto I) {
      kread(IC<I.v + 2>{}, I);
      if constexpr (MODE != 2) mfma_s(snext, kf[I.v], qf[I.v]);
      if constexpr (I.v == 1) mx = max3(max3(max3(max3(scur[0], scur[1], scur[2]), scur[3], scur[4]), scur[5], scur[6]), scur[7], scur[8]);
      if constexpr (I.v == 2) {
        mx = max3(max3(max3(max3(mx, scur[9], scur[10]), scur[11], scur[12]), scur[13], scur[14]), scur[15], scur[15]);
        const unsigned u = __builtin_bit_cast(unsigned, mx);
        const auto sw = __builtin_amdgcn_permlane32_swap(u, u, false, false);
        mx = max3(__builtin_bit_cast(float, (unsigned)sw[0]), __builtin_bit_cast(float, (unsigned)sw[1]), mx);
      }
      stream(I);
      SB();
    });
    if (jj == 0 || __any(mx > 64.f)) {
      const float mn = mrun + ((jj == 0) ? mx : fmaxf(mx, 0.f));
      const unsigned mh = pack2(mn, 0.f) & 0xffffu, ml = pack2(mn - bflo(mh), 0.f) & 0xffffu;
      const float dl = (bflo(mh) + bflo(ml)) - mrun;
      mrun = bflo(mh) + bflo(ml);
      cb[0] = h ? 0u : (0xff610000u | (mh ^ 0x8000u));
      cb[1] = h ? 0u : (ml ^ 0x8000u);
#pragma unroll
      for (int i = 0; i < 16; ++i) scur[i] -= dl;
      if constexpr (MODE != 2) {
        asm volatile("s_nop 7\n\ts_nop 7\n\tv_sub_f32 %0, %0, %1" : "+v"(snext[0]) : "v"(dl));
#pragma unroll
        for (int i = 1; i < 15; ++i) asm volatile("v_sub_f32 %0, %0, %1" : "+v"(snext[i]) : "v"(dl));
        asm volatile("v_sub_f32 %0, %0, %1\n\ts_nop 1" : "+v"(snext[15]) : "v"(dl));
      }
      if (jj != 0) {
        const float f = __builtin_amdgcn_exp2f(-dl);
        lsum *= f;
#pragma unroll
        for (int c = 0; c < 12; ++c)
#pragma unroll
          for (int i = 0; i < 16; ++i) {
            float tmp;
            asm volatile("v_accvgpr_read_b32 %1, %0\n\tv_mul_f32 %1, %1, %2\n\tv_accvgpr_write_b32 %0, %1" : "+a"(o[c][i]), "=&v"(tmp) : "v"(f));
          }
        asm volatile("s_nop 4");
      }
    }
    SB();
    bf16x8 pf0, pf1;
    sfor<3, 8>([&](auto I) {
      constexpr int f = I.v;
      if constexpr (f + 2 < 8) kread(IC<f + 2>{}, I);
      if constexpr (f == 4) { tread(IC<1>{}); tread(IC<0>{}); }
      if constexpr (f == 6) { tread(IC<3>{}); tread(IC<2>{}); }
      if constexpr (MODE != 2) mfma_s(snext, kf[f], qf[f]);
      if constexpr (f < 7) {
        scur[2 * (f - 3)] = __builtin_amdgcn_exp2f(scur[2 * (f - 3)]); scur[2 * (f - 3) + 1] = __builtin_amdgcn_exp2f(scur[2 * (f - 3) + 1]);
      } else pf0 = pack8(scur, 0);
      stream(I);
      SB();
    });
    auto oput = [&](auto I) {
      constexpr int u = I.v, k = 3 * (u >> 2) + (u & 3) - 1;
      if constexpr ((u & 3) != 0) {
        char* dst = ((k < 4) ? nb[k >> 1] : rb[(k - 4) >> 1]) + (k & 1) * 4096 + l * 16;
#pragma unroll
        for (int g = 0; g < 4; ++g) *(f32x4*)(dst + g * 1024) = f32x4{o[u][4 * g], o[u][4 * g + 1], o[u][4 * g + 2], o[u][4 * g + 3]};
      }
    };
    const char* srcC = srcB;
    sfor<0, 24>([&](auto I) {
      constexpr int p = I.v;
      if constexpr ((p & 1) == 0 && p + PFD < 24) { tread(IC<p + PFD + 1>{}); tread(IC<p + PFD>{}); }
      if constexpr (p >= 22) kread(IC<p - 14>{}, IC<8 + p>{});
      o[pv_u(p)] = MFMA(tf[p], pv_s(p) ? pf1 : pf0, o[pv_u(p)]);
      if constexpr (p < 4) {
        scur[8 + 2 * p] = __builtin_amdgcn_exp2f(scur[8 + 2 * p]); scur[9 + 2 * p] = __builtin_amdgcn_exp2f(scur[9 + 2 * p]);
        if constexpr (p == 3) pf1 = pack8(scur, 1);
      } else if constexpr (p < 12) lsum += f32x2{scur[2 * (p - 4)], scur[2 * (p - 4) + 1]};
      if constexpr (MODE == 0 && p == 13) srcC = keysm + (size_t)tile_of(jj + 3) * TILEB;
      stream(IC<8 + p>{});
      if constexpr (MODE == 2 && p >= 7 && p < 10) oput(IC<p - 6>{});
      if constexpr (MODE == 2 && p >= 13 && (p & 1)) oput(IC<(p - 3) / 2>{});
      SB();
    });
    if constexpr (MODE == 2) oput(IC<11>{});
    else {
      sfor<8, 24>([&](auto I) {
        constexpr int f = I.v;
        kread(IC<f + 2>{}, IC<24 + f>{});
        if constexpr (f < 23) { mfma_s(snext, kf[f], qf[f]); stream(IC<24 + f>{}); }
        else { stream(IC<24 + f>{}); mfma_s_move<MODE == 1>(scur, snext, kf[f], qf[f]); }
        SB();
      });
      if constexpr (MODE == 0) { kc[0] = kf[24]; kc[1] = kf[25]; }
      const int t0 = sg0, t1 = sg1, t2 = sg2;
      sg0 = sg3; sg1 = sg4; sg2 = t0; sg3 = t1; sg4 = t2;
      srcA = srcB; srcB = srcC;
    }
  };
  {
    int jj = 0;
    do period(IC<0>{}, jj); while (++jj < nt - 2);
  }
  period(IC<1>{}, nt - 2);
  period(IC<2>{}, nt - 1);

  const float ltot = xsum(lsum[0] + lsum[1]);
  auto oslot = [&](int sw, int k) {
    const int b = (sw < 3) ? 0 : 4;
    const int i = b + k + 6;
    return lds + sw * 40960 + ((i >= 10) ? i - 10 : i) * 4096;
  };
  if (h == 0) *(f32x2*)(oslot(w, 9) + r * 8) = f32x2{mrun, ltot};
  SB();
  sfor<52, 54>(w2issue);
  lds_barrier();
  float fs[4];
  {
    const f32x2 a = *(const f32x2*)(oslot(w, 9) + r * 8), b = *(const f32x2*)(oslot(w ^ 1, 9) + r * 8);
    const f32x2 c = *(const f32x2*)(oslot(w ^ 2, 9) + r * 8), d = *(const f32x2*)(oslot(w ^ 3, 9) + r * 8);
    const float m = fmaxf(fmaxf(a[0], b[0]), fmaxf(c[0], d[0]));
    fs[0] = __builtin_amdgcn_exp2f(a[0] - m); fs[1] = __builtin_amdgcn_exp2f(b[0] - m);
    fs[2] = __builtin_amdgcn_exp2f(c[0] - m); fs[3] = __builtin_amdgcn_exp2f(d[0] - m);
    const float inv = __builtin_amdgcn_rcpf(a[1] * fs[0] + b[1] * fs[1] + c[1] * fs[2] + d[1] * fs[3]);
    fs[0] *= inv; fs[1] *= inv; fs[2] *= inv; fs[3] *= inv;
  }
  f32x4 pa[4][3];
  u32x4 opk[2];
  auto mread = [&](auto SG, auto G) {
#pragma unroll
    for (int cc = 1; cc < 4; ++cc) pa[G.v][cc - 1] = *(const f32x4*)(oslot(w ^ cc, 3 * SG.v + cc - 1) + l * 16 + G.v * 1024);
  };
  auto mstep = [&](auto SG, auto G) {
    constexpr int sg = SG.v, g = G.v;
    f32x4 own;
#pragma unroll
    for (int e = 0; e < 4; ++e) {
      float tmp;
      asm volatile("v_accvgpr_read_b32 %0, %1" : "=v"(tmp) : "a"(o[4 * sg][4 * g + e]));
      own[e] = tmp;
    }
    own = own * fs[0] + pa[g][0] * fs[1] + pa[g][1] * fs[2] + pa[g][2] * fs[3];
    opk[g >> 1][2 * (g & 1)] = pack2(own[0], own[1]); opk[g >> 1][2 * (g & 1) + 1] = pack2(own[2], own[3]);
    if constexpr (g == 3) {
      char* dst = oslot(w ^ 1, 3 * sg) + l * 16;
      *(u32x4*)dst = opk[0]; *(u32x4*)(dst + 1024) = opk[1];
    }
  };
  sfor<0, 4>([&](auto G) { mread(IC<0>{}, G); });
  SB();
  sfor<54, 56>(w2issue);
  sfor<0, 4>([&](auto G) { mstep(IC<0>{}, G); });
  lds_barrier();
  f32x16 acc[3] = {};
  u32x4 xv[6];
  f32x2 ms;
  {
    const char* x1 = H2 + ((size_t)t * NTILE + qt) * TILEB + (size_t)(6 * w) * 1024 + 2048;
    const char* xa = x1 + so0;
    const char* xb = x1 + so1;
    bf16x8 of[24];
    auto oread = [&](auto I) {
      constexpr int ks = I.v;
      of[ks] = *(const bf16x8*)(oslot(((ks >> 1) & 3) ^ 1, 3 * (ks >> 3)) + (ks & 1) * 1024 + l * 16);
    };
    sfor<0, 3>([&](auto SG) {
      constexpr int sg = SG.v;
      sfor<8 * sg, 8 * sg + 2>(oread);
      SB();
      sfor<0, 8>([&](auto K) {
        constexpr int k = K.v, ks = 8 * sg + k;
        if constexpr (k + 2 < 8) oread(IC<ks + 2>{});
        if constexpr (sg < 2 && k < 4) mread(IC<sg + 1>{}, K);
        if constexpr (ks < 8) sfor<56 + 2 * ks, 58 + 2 * ks>(w2issue);
        else if constexpr (ks < 14) gload_p<(ks - 8) * 1024 - 2048>(xv[ks - 8], ((ks - 8) & 1) ? xb : xa);
        else if constexpr (ks == 14)
          asm volatile("global_load_dwordx2 %0, %1, off" : "=v"(ms) : "v"(X1 + ((size_t)t * (32 * NTILE) + 32 * qt + r) * 8));
        constexpr int issued = (ks < 8) ? 58 + 2 * ks : ((ks < 15) ? 72 + (ks - 7) : 79);
        wwait<issued - 3 * ks - 3, w2a(3 * ks + 2)>(w2[3 * ks + 2]); wtie<w2a(3 * ks)>(w2[3 * ks]); wtie<w2a(3 * ks + 1)>(w2[3 * ks + 1]);
#pragma unroll
        for (int i = 0; i < 3; ++i) acc[i] = MFMA(w2[3 * ks + i], of[ks], acc[i]);
        if constexpr (sg < 2 && k >= 2 && k < 6) mstep(IC<sg + 1>{}, IC<k - 2>{});
        SB();
      });
      if constexpr (sg < 2) lds_barrier();
    });
  }
  wait6<0>(xv);
  asm volatile("" : "+v"(ms));
  {
    char* ob0 = oslot(w ^ 2, 1);
    char* ob1 = oslot(w ^ 3, 2) - 16 * 208;
#pragma unroll
    for (int i = 0; i < 6; ++i) {
      u32x4 p;
#pragma unroll
      for (int j = 0; j < 4; ++j)
        p[j] = pk(unpk(xv[i][j]) * ms[1] + (ms[0] + f32x2{acc[i >> 1][8 * (i & 1) + 2 * j], acc[i >> 1][8 * (i & 1) + 2 * j + 1]}));
      *(u32x4*)(((r < 16) ? ob0 : ob1) + r * 208 + i * 32 + h * 16) = p;
    }
    unsigned short* obase = out + ((size_t)t * NP + 32 * qt) * CH + 96 * w;
#pragma unroll
    for (int i = 0; i < 6; ++i) {
      const int id = i * 64 + l, row = id / 12, ch = id - 12 * row;
      const u32x4 p = *(const u32x4*)(((i < 3) ? ob0 : ob1) + row * 208 + ch * 16);
      if (32 * qt + row < NP) *(u32x4*)(obase + (size_t)row * CH + 8 * ch) = p;
    }
  }
}

extern "C" void kernel_launch(void* const* d_in, const int* in_sizes, int n_in,
                              void* d_out, int out_size, void* d_ws, size_t ws_size,
                              hipStream_t stream) {
  const unsigned short* x = (const unsigned short*)d_in[0];
  const unsigned short* wqkv_t = (const unsigned short*)d_in[1];
  const unsigned short* wo_t = (const unsigned short*)d_in[2];
  const unsigned short* wqkv_s = (const unsigned short*)d_in[3];
  const unsigned short* wo_s = (const unsigned short*)d_in[4];
  char* ws = (char*)d_ws;
  k0_fold<<<144, 256, 0, stream>>>(wqkv_t, wo_t, wqkv_s, wo_s, ws);
  k1_temporal<<<184, 768, 0, stream>>>(x, ws + OFF_WA_T, ws + OFF_WB_T, ws + OFF_H2, ws + OFF_X1);
  k2_spatial<<<184, 256, 0, stream>>>(ws + OFF_WA_S, ws + OFF_WB_S, ws + OFF_H2, ws + OFF_X1, (unsigned short*)d_out);
}
```
